```python
import torch

def _softmax(x, axis=-1):
    return torch.softmax(x, dim=axis)
def _gelu(x):
    return torch.nn.functional.gelu(x, approximate="tanh")
def _silu(x):
    return torch.nn.functional.silu(x)
def _sigmoid(x):
    return torch.sigmoid(x)
def _layernorm(x, axis=-1, eps=1e-5):
    m = x.mean(dim=axis, keepdim=True)
    v = x.var(dim=axis, keepdim=True, unbiased=False)
    return (x - m) / torch.sqrt(v + eps)
def _scatter_add(tgt, idx, src, axis=0):
    return tgt.clone().index_add_(axis, idx, src)
def _segment_sum(src, seg, n):
    out = torch.zeros((n,) + src.shape[1:], dtype=src.dtype)
    return out.index_add_(0, seg, src)
def _pad(x, pw):
    flat = tuple(v for pair in reversed(pw) for v in pair)
    return torch.nn.functional.pad(x, flat)

kernel_name = 'concat_heads__softmax_full_mha__sliding_local_dilated__prompt'

def setup_inputs():

    _w = (lambda a: (lambda t: t.to(torch.bfloat16) if t.is_floating_point() else t)(torch.as_tensor(a)))
    def _A_setup():
        B, S, D = 1, 2048, 448
        return {
            "x": _w((lambda *s: torch.randn(*s, dtype=torch.bfloat16) * 0.2)(B, S, D)),
            "wqkv": _w((lambda *s: torch.randn(*s, dtype=torch.bfloat16) * 0.8)(D, 3 * D)),
            "wo": _w((lambda *s: torch.randn(*s, dtype=torch.bfloat16) * 0.2)(D, D)),
        }
    _A_in = _A_setup()

    def _B_setup():
        B, S, D = 1, 2048, 448
        return {
            "x": _w((lambda *s: torch.randn(*s, dtype=torch.bfloat16) * 0.4)(B, S, D)),
            "wqkv": _w((lambda *s: torch.randn(*s, dtype=torch.bfloat16) * 0.8)(D, 3 * D)),
            "wo": _w((lambda *s: torch.randn(*s, dtype=torch.bfloat16) * 0.1)(D, D)),
        }
    _B_in = _B_setup()

    return {
        "A_x": _A_in["x"],
        "A_wqkv": _A_in["wqkv"],
        "A_wo": _A_in["wo"],
        "B_x": _B_in["x"],
        "B_wqkv": _B_in["wqkv"],
        "B_wo": _B_in["wo"],
    }

def reference(A_x, A_wqkv, A_wo, B_x, B_wqkv, B_wo):
    def _A_ref(x, wqkv, wo):
        B, S, D = x.shape
        H = 16; Dh = D // H
        qkv = (lambda x, *d: x.permute(*d))((x @ wqkv).reshape(B, S, 3, H, Dh), 2, 0, 3, 1, 4)
        q, k, v = qkv[0], qkv[1], qkv[2]
        scale = Dh ** -0.5
        scores = torch.einsum("bhqd,bhkd->bhqk", q, k) * scale
        p = _softmax(scores, axis=-1)
        o = torch.einsum("bhqk,bhkd->bhqd", p, v)
        return (lambda x, *d: x.permute(*d))(o, 0, 2, 1, 3).reshape(B, S, D) @ wo

    def _B_ref(x, wqkv, wo):
        B, S, D = x.shape
        H = 16; Dh = D // H
        W, dil = 8, 2
        qkv = (lambda x, *d: x.permute(*d))((x @ wqkv).reshape(B, S, 3, H, Dh), 2, 0, 3, 1, 4)
        q, k, v = qkv[0], qkv[1], qkv[2]
        scale = Dh ** -0.5
        pos = torch.arange(S)
        off = torch.arange(W) * dil
        idx = pos[:, None] - off[None, :]
        mask = idx >= 0
        idx_c = torch.where(mask, idx, 0).reshape(-1)
        kg = k[:, :, idx_c, :].reshape(B, H, S, W, Dh)
        vg = v[:, :, idx_c, :].reshape(B, H, S, W, Dh)
        logits = torch.einsum("bhsd,bhswd->bhsw", q, kg) * scale
        logits = torch.where(mask[None, None, :, :], logits, -1e9)
        attn = _softmax(logits, axis=-1)
        o = torch.einsum("bhsw,bhswd->bhsd", attn, vg)
        return (lambda x, *d: x.permute(*d))(o, 0, 2, 1, 3).reshape(B, S, D) @ wo

    a = _A_ref(A_x, A_wqkv, A_wo)
    b = _B_ref(B_x, B_wqkv, B_wo)
    return torch.concatenate([a, b], axis=-1)
```

```cpp
#include <hip/hip_runtime.h>
#include <hip/hip_bf16.h>
#include <stdint.h>
typedef unsigned short u16;
#define MIMIC 0
__device__ __forceinline__ float bf2f(u16 b){ return __uint_as_float(((unsigned)b)<<16); }
__device__ __forceinline__ u16 f2bf(float f){ unsigned u=__float_as_uint(f); u += 0x7fffu + ((u>>16)&1u); return (u16)(u>>16); }
__device__ __forceinline__ float rbf(float f){ return bf2f(f2bf(f)); }

__global__ void gemm_naive(const u16* A, const u16* B, u16* C, int M, int N, int K, int ldc, int coff){
  int n = blockIdx.x*blockDim.x+threadIdx.x; int m = blockIdx.y;
  if(n>=N) return;
  float acc=0.f;
  for(int k=0;k<K;++k) acc += bf2f(A[m*K+k])*bf2f(B[k*N+n]);
  C[m*ldc+coff+n]=f2bf(acc);
}
__global__ void attnA(const u16* qkv, u16* o){
  int idx = blockIdx.x*blockDim.x+threadIdx.x;
  int h = idx>>11, s = idx&2047;
  float q[28];
  #pragma unroll
  for(int d=0;d<28;++d) q[d]=bf2f(qkv[s*1344+h*28+d]);
  const float scale = 0.1889822365046136f;
  float m=-INFINITY;
  for(int j=0;j<2048;++j){
    float a=0.f;
    #pragma unroll
    for(int d=0;d<28;++d) a+=q[d]*bf2f(qkv[j*1344+448+h*28+d]);
    if(MIMIC){ a=rbf(rbf(a)*scale);} else a*=scale;
    m=fmaxf(m,a);
  }
  float l=0.f;
  if(MIMIC){
  for(int j=0;j<2048;++j){
    float a=0.f;
    #pragma unroll
    for(int d=0;d<28;++d) a+=q[d]*bf2f(qkv[j*1344+448+h*28+d]);
    a=rbf(rbf(a)*scale);
    l+=__expf(a-m);
  }}
  float acc[28];
  #pragma unroll
  for(int d=0;d<28;++d) acc[d]=0.f;
  float l2=0.f;
  for(int j=0;j<2048;++j){
    float a=0.f;
    #pragma unroll
    for(int d=0;d<28;++d) a+=q[d]*bf2f(qkv[j*1344+448+h*28+d]);
    if(MIMIC){ a=rbf(rbf(a)*scale);} else a*=scale;
    float p=__expf(a-m);
    l2+=p;
    if(MIMIC) p=rbf(p/l); else p = rbf(p);
    #pragma unroll
    for(int d=0;d<28;++d) acc[d]+=p*bf2f(qkv[j*1344+896+h*28+d]);
  }
  #pragma unroll
  for(int d=0;d<28;++d) o[s*448+h*28+d]=f2bf(MIMIC?acc[d]:acc[d]/l2);
}
__global__ void attnB(const u16* qkv, u16* o){
  int idx = blockIdx.x*blockDim.x+threadIdx.x;
  int h = idx>>11, s = idx&2047;
  const float scale = 0.1889822365046136f;
  float lg[8]; float m=-INFINITY;
  #pragma unroll
  for(int w=0;w<8;++w){
    int j=s-2*w; bool ok=j>=0; if(!ok) j=0;
    float a=0.f;
    for(int d=0;d<28;++d) a+=bf2f(qkv[s*1344+h*28+d])*bf2f(qkv[j*1344+448+h*28+d]);
    if(MIMIC){ a=rbf(rbf(a)*scale);} else a*=scale;
    if(!ok) a=-1e9f;
    lg[w]=a; m=fmaxf(m,a);
  }
  float l=0.f;
  #pragma unroll
  for(int w=0;w<8;++w){ lg[w]=__expf(lg[w]-m); l+=lg[w]; }
  #pragma unroll
  for(int w=0;w<8;++w){ lg[w]=lg[w]/l; if(MIMIC) lg[w]=rbf(lg[w]); }
  for(int d=0;d<28;++d){
    float a=0.f;
    #pragma unroll
    for(int w=0;w<8;++w){ int j=s-2*w; if(j<0) j=0; a+=lg[w]*bf2f(qkv[j*1344+896+h*28+d]); }
    o[s*448+h*28+d]=f2bf(a);
  }
}
extern "C" void kernel_launch(void* const* d_in, const int* in_sizes, int n_in,
                              void* d_out, int out_size, void* d_ws, size_t ws_size,
                              hipStream_t stream) {
  const u16* Ax=(const u16*)d_in[0]; const u16* Aw=(const u16*)d_in[1]; const u16* Ao=(const u16*)d_in[2];
  const u16* Bx=(const u16*)d_in[3]; const u16* Bw=(const u16*)d_in[4]; const u16* Bo=(const u16*)d_in[5];
  u16* out=(u16*)d_out; u16* ws=(u16*)d_ws;
  u16* qkvA=ws; u16* qkvB=ws+2048*1344; u16* oA=qkvB+2048*1344; u16* oB=oA+2048*448;
  gemm_naive<<<dim3(1344/64,2048),64,0,stream>>>(Ax,Aw,qkvA,2048,1344,448,1344,0);
  gemm_naive<<<dim3(1344/64,2048),64,0,stream>>>(Bx,Bw,qkvB,2048,1344,448,1344,0);
  attnA<<<32768/64,64,0,stream>>>(qkvA,oA);
  attnB<<<32768/64,64,0,stream>>>(qkvB,oB);
  gemm_naive<<<dim3(448/64,2048),64,0,stream>>>(oA,Ao,out,2048,448,448,896,0);
  gemm_naive<<<dim3(448/64,2048),64,0,stream>>>(oB,Bo,out,2048,448,448,896,448);
}
```

```cpp
#include <hip/hip_runtime.h>
#include <stdint.h>

typedef unsigned short u16;
typedef short bf16x8 __attribute__((ext_vector_type(8)));
typedef short s16x4 __attribute__((ext_vector_type(4)));
typedef float f32x16 __attribute__((ext_vector_type(16)));
typedef float f32x4 __attribute__((ext_vector_type(4)));
typedef float f32x2 __attribute__((ext_vector_type(2)));
typedef unsigned u32x4 __attribute__((ext_vector_type(4)));
typedef unsigned u32x2 __attribute__((ext_vector_type(2)));
typedef __bf16 bf2_t __attribute__((ext_vector_type(2)));
#define LAS __attribute__((address_space(3)))
typedef LAS char* lptr;
#define DI __device__ __forceinline__
#define MFMA(a, b, c) __builtin_amdgcn_mfma_f32_32x32x16_bf16((a), (b), (c), 0, 0, 0)
#define SBAR() __builtin_amdgcn_sched_barrier(0)
#define PIN(x) asm volatile("" : "+v"(x))

constexpr int XROW = 896, WROW = 2688, OROW = 1792;
constexpr float QS = 0.18898223650461362f * 1.4426950408889634f;
constexpr unsigned WS_Q = 0, WS_KV = 2u << 20, WS_BR = 6u << 20, WS_O = 12u << 20, WS_OBR = 2u << 20, WS_END = 17u << 20;
constexpr unsigned SENT = 0xFFFFFFFFu;
constexpr int LDS_BYTES = 160 * 1024;

#define ST_DECL
#define STAMP(i)
#define RSTAMP(i)
#define ST_OUT(ph)

DI unsigned pk(float a, float b) { f32x2 v = {a, b}; return __builtin_bit_cast(unsigned, __builtin_convertvector(v, bf2_t)); }
DI bf16x8 pack8(const f32x16& x, int s) {
    u32x4 p = {pk(x[8 * s], x[8 * s + 1]), pk(x[8 * s + 2], x[8 * s + 3]), pk(x[8 * s + 4], x[8 * s + 5]), pk(x[8 * s + 6], x[8 * s + 7])};
    return __builtin_bit_cast(bf16x8, p);
}
DI void glds(const void* sbase, unsigned voff, unsigned ldst) {
    unsigned sv;
    asm volatile("s_mov_b32 %0, m0\n\ts_mov_b32 m0, %3\n\ts_nop 2\n\tglobal_load_lds_dwordx4 %1, %2\n\ts_mov_b32 m0, %0"
                 : "=&s"(sv) : "v"(voff), "s"(sbase), "s"(ldst) : "memory");
}
template <int N> DI void wait_vm() { asm volatile("s_waitcnt vmcnt(%0)" ::"n"(N) : "memory"); }
DI void bar() { asm volatile("s_waitcnt lgkmcnt(0)\n\ts_barrier" ::: "memory"); }
DI bf16x8 lds16(lptr p) { return *(const LAS bf16x8*)p; }
typedef short v4i16_t __attribute__((ext_vector_type(4)));
DI s16x4 ldtr(lptr p) { return __builtin_bit_cast(s16x4, __builtin_amdgcn_ds_read_tr16_b64_v4i16((LAS v4i16_t*)p)); }
DI bf16x8 ldtr8(lptr p, int stride4) { s16x4 lo = ldtr(p), hi = ldtr(p + stride4); return __builtin_shufflevector(lo, hi, 0, 1, 2, 3, 4, 5, 6, 7); }
DI float swap_hi(float x) { auto r = __builtin_amdgcn_permlane32_swap(__float_as_uint(x), __float_as_uint(x), false, false); return __uint_as_float(r[1]); }
DI float halfmax(float x) { auto r = __builtin_amdgcn_permlane32_swap(__float_as_uint(x), __float_as_uint(x), false, false); return __builtin_fmaxf(__uint_as_float(r[0]), __uint_as_float(r[1])); }
DI float max16(const f32x16& p) {
    float a = __builtin_fmaxf(__builtin_fmaxf(p[0], p[1]), p[2]), b = __builtin_fmaxf(__builtin_fmaxf(p[3], p[4]), p[5]);
    a = __builtin_fmaxf(__builtin_fmaxf(a, p[6]), p[7]); b = __builtin_fmaxf(__builtin_fmaxf(b, p[8]), p[9]);
    a = __builtin_fmaxf(__builtin_fmaxf(a, p[10]), p[11]); b = __builtin_fmaxf(__builtin_fmaxf(b, p[12]), p[13]);
    a = __builtin_fmaxf(__builtin_fmaxf(a, p[14]), p[15]);
    return __builtin_fmaxf(a, b);
}
typedef __amdgpu_buffer_rsrc_t rsrc_t;
DI rsrc_t ws_rsrc(char* ws) { return __builtin_amdgcn_make_buffer_rsrc(ws, 0, WS_END, 0x00020000); }
DI void st_handoff(rsrc_t rs, unsigned off, bf16x8 v) {
    u32x4 u = __builtin_bit_cast(u32x4, v);
#pragma unroll
    for (int i = 0; i < 4; ++i) u[i] = u[i] == SENT ? 0x7FFF7FFFu : u[i];
    __builtin_amdgcn_raw_buffer_store_b128(u, rs, off, 0, 16);
}

struct Params { const char *xa, *wa, *woa, *xb, *wb, *wob; char* out; char* ws; };

constexpr int K1_XB = 16 * 1024, K1_STAGE = K1_XB + 21 * 1024, K1_NST = 4;
DI void k1_store(char* wsb, f32x16 a, int slot, int rb, int lane, float qs) {
    const int hh = lane >> 5;
    char* dst;
    if (slot < 16) {
#pragma unroll
        for (int r = 0; r < 16; ++r) a[r] *= qs;
        if (hh) { a[12] = 0.f; a[13] = 0.f; a[14] = 0.f; a[15] = 0.f; }
        dst = wsb + WS_Q + (size_t)((slot * 64 + rb) * 2) * 1024;
    } else if (slot < 32) {
        if (hh) { a[12] = 1.f; a[13] = 0.f; a[14] = 0.f; a[15] = 0.f; }
        dst = wsb + WS_KV + (size_t)(((slot - 16) * 64 + rb) * 4) * 1024;
    } else {
        const int d = lane & 31;
        if (d >= 28) {
            const float c = d == 28 ? 1.f : 0.f;
#pragma unroll
            for (int r = 0; r < 16; ++r) a[r] = c;
        }
        dst = wsb + WS_KV + (size_t)(((slot - 32) * 64 + rb) * 4 + 2) * 1024;
    }
    *(bf16x8*)(dst + lane * 16) = pack8(a, 0);
    *(bf16x8*)(dst + 1024 + lane * 16) = pack8(a, 1);
}

template <bool V2, bool V01>
DI void k1_chunk(lptr xs, int cx, lptr ws_, f32x16& a0, f32x16& a1, f32x16& a2) {
#pragma unroll
    for (int ks = 0; ks < 4; ++ks) {
        const bf16x8 xf = lds16(xs + ((cx ^ (2 * ks)) << 4));
        const bf16x8 w0 = ldtr8(ws_ + ks * 16 * 336, 336), w1 = ldtr8(ws_ + ks * 16 * 336 + 56, 336), w2 = ldtr8(ws_ + ks * 16 * 336 + 112, 336);
        a0 = V01 ? MFMA(xf, w0, a0) : MFMA(w0, xf, a0);
        a1 = V01 ? MFMA(xf, w1, a1) : MFMA(w1, xf, a1);
        a2 = V2 ? MFMA(xf, w2, a2) : MFMA(w2, xf, a2);
    }
}

__global__ __launch_bounds__(1024, 4) void k_qkv(Params p) {
    extern __shared__ __attribute__((aligned(16))) char ldsg[];
    const int tid = threadIdx.x, lane = tid & 63, wv = __builtin_amdgcn_readfirstlane(tid >> 6), w = wv & 7, b = blockIdx.x;
    ST_DECL; STAMP(0); RSTAMP(22);
    const int xcd = b & 7, j = b >> 3, br = xcd >> 2, x2 = xcd & 3, mt = (x2 & 1) * 8 + (j & 7), g = (x2 >> 1) * 4 + (j >> 3);
    lptr lds = (lptr)ldsg; const unsigned lds0 = (unsigned)(uintptr_t)ldsg;
    if (wv >= 8) {
        const char* xg = (br ? p.xb : p.xa) + (size_t)mt * 128 * XROW;
        const char* wg = (br ? p.wb : p.wa) + g * 336;
        unsigned voff[5], ldst[5];
        auto wpiece = [&](int n, unsigned& vo, unsigned& ld) { const int ci = n * 64 + lane, rho = ci / 21, c = ci - rho * 21, k = (rho & ~15) + 4 * (rho & 3) + ((rho >> 2) & 3);
            vo = k * WROW + c * 16; ld = K1_XB + n * 1024; };
        if (w < 4) {
#pragma unroll
            for (int i = 0; i < 4; ++i) { const int r = w * 32 + 8 * i + (lane >> 3); voff[i] = r * XROW + (((lane & 7) ^ ((r >> 1) & 7)) << 4); ldst[i] = (w * 4 + i) * 1024; }
            wpiece(w, voff[4], ldst[4]);
        } else {
            const int n0 = w == 4 ? 4 : 4 * w - 11;
#pragma unroll
            for (int i = 0; i < 4; ++i) wpiece(n0 + i, voff[i], ldst[i]);
            wpiece(8, voff[4], ldst[4]);
        }
        const char* sb03 = w < 4 ? xg : wg; const unsigned st03 = w < 4 ? 128u : 64u * WROW;
        const bool five = w <= 4;
#define K1_ISSUE(kc) do { const char* b_ = sb03 + (size_t)(kc) * st03; const unsigned l_ = lds0 + ((kc) % K1_NST) * K1_STAGE; \
        glds(b_, voff[0], l_ + ldst[0]); glds(b_, voff[1], l_ + ldst[1]); glds(b_, voff[2], l_ + ldst[2]); glds(b_, voff[3], l_ + ldst[3]); \
        if (five) glds(wg + (size_t)(kc) * 64 * WROW, voff[4], l_ + ldst[4]); } while (0)
        K1_ISSUE(0); K1_ISSUE(1); K1_ISSUE(2);
        STAMP(1);
#define K1_LSTEP(kc, AHEAD) do { if (five) wait_vm<5 * (AHEAD)>(); else wait_vm<4 * (AHEAD)>(); STAMP(4 + kc); bar(); if ((kc) + 3 < 7) K1_ISSUE((kc) + 3); } while (0)
        K1_LSTEP(0, 2); K1_LSTEP(1, 2); K1_LSTEP(2, 2); K1_LSTEP(3, 2); K1_LSTEP(4, 2); K1_LSTEP(5, 1); K1_LSTEP(6, 0);
#undef K1_ISSUE
#undef K1_LSTEP
        { u32x4 s4 = {SENT, SENT, SENT, SENT}; char* d = p.ws + WS_O + (size_t)b * 16384 + w * 2048 + lane * 16; *(u32x4*)d = s4; *(u32x4*)(d + 1024) = s4; }
        STAMP(3); RSTAMP(23); ST_OUT(0);
        return;
    }
    const int r = lane & 31, hh = lane >> 5, blk = (lane >> 4) & 1, q = (lane & 15) >> 2, pp = lane & 3;
    const int rbl = w & 3, tr = w >> 2, T = 2 * g + tr;
    const int xrow = (rbl * 32 + r) * 128, cx = hh ^ ((r >> 1) & 7);
    const int wl = K1_XB + (4 * q + 2 * hh) * 336 + 168 * tr + 32 * blk + 8 * pp;
    f32x16 a0 = {}, a1 = {}, a2 = {};
    STAMP(1);
#define K1_STEP(kc, V2, V01) do { bar(); STAMP(4 + kc); { lptr s_ = lds + ((kc) % K1_NST) * K1_STAGE; k1_chunk<V2, V01>(s_ + xrow, cx, s_ + wl, a0, a1, a2); } } while (0)
#define K1_LOOP(V2, V01) do { K1_STEP(0, V2, V01); K1_STEP(1, V2, V01); K1_STEP(2, V2, V01); K1_STEP(3, V2, V01); K1_STEP(4, V2, V01); K1_STEP(5, V2, V01); K1_STEP(6, V2, V01); } while (0)
    if (T >= 11) K1_LOOP(true, true); else if (T == 10) K1_LOOP(true, false); else K1_LOOP(false, false);
    STAMP(2);
    char* wsb = p.ws + (br ? WS_BR : 0u); const float qs = br ? 1.f : QS; const int rb = mt * 4 + rbl;
    k1_store(wsb, a0, 3 * T, rb, lane, qs); k1_store(wsb, a1, 3 * T + 1, rb, lane, qs); k1_store(wsb, a2, 3 * T + 2, rb, lane, qs);
    STAMP(3); RSTAMP(23); ST_OUT(0);
#undef K1_STEP
#undef K1_LOOP
}

DI void local_unit(const Params& p, rsrc_t rs, int rb, int h, int lane) {
    const int r = lane & 31, hh = lane >> 5, rp = rb > 0 ? rb - 1 : 0;
    const char* qsrc = p.ws + WS_BR + WS_Q + (size_t)((h * 64 + rb) * 2) * 1024 + lane * 16;
    const char* kc = p.ws + WS_BR + WS_KV + (size_t)((h * 64 + rb) * 4) * 1024 + lane * 16;
    const char* kp = p.ws + WS_BR + WS_KV + (size_t)((h * 64 + rp) * 4) * 1024 + lane * 16;
    const bf16x8 q0 = *(const bf16x8*)qsrc, q1 = *(const bf16x8*)(qsrc + 1024);
    const bf16x8 kp0 = *(const bf16x8*)kp, kp1 = *(const bf16x8*)(kp + 1024), kc0 = *(const bf16x8*)kc, kc1 = *(const bf16x8*)(kc + 1024);
    const bf16x8 vp0 = *(const bf16x8*)(kp + 2048), vp1 = *(const bf16x8*)(kp + 3072), vc0 = *(const bf16x8*)(kc + 2048), vc1 = *(const bf16x8*)(kc + 3072);
    f32x16 sp = {}, sc = {};
    sp = MFMA(kp0, q0, sp); sc = MFMA(kc0, q0, sc); sp = MFMA(kp1, q1, sp); sc = MFMA(kc1, q1, sc);
    const int dbase = r - 4 * hh;
#pragma unroll
    for (int i = 0; i < 16; ++i) {
        const int dc = dbase - ((i & 3) + 8 * (i >> 2)), dp = dc + 32;
        sc[i] = ((unsigned)dc <= 14u && !(dc & 1)) ? sc[i] * QS : -INFINITY;
        sp[i] = (rb > 0 && dp <= 14 && !(dp & 1)) ? sp[i] * QS : -INFINITY;
    }
    const float m = halfmax(__builtin_fmaxf(max16(sp), max16(sc)));
#pragma unroll
    for (int i = 0; i < 16; ++i) { sp[i] = __builtin_amdgcn_exp2f(sp[i] - m); sc[i] = __builtin_amdgcn_exp2f(sc[i] - m); }
    f32x16 o = {};
    o = MFMA(vp0, pack8(sp, 0), o); o = MFMA(vp1, pack8(sp, 1), o); o = MFMA(vc0, pack8(sc, 0), o); o = MFMA(vc1, pack8(sc, 1), o);
    const float inv = 1.f / swap_hi(o[12]);
#pragma unroll
    for (int i = 0; i < 16; ++i) o[i] *= inv;
    if (hh) { o[12] = o[13] = o[14] = o[15] = 0.f; }
    const unsigned dst = WS_O + WS_OBR + ((rb * 16 + h) * 2) * 1024 + lane * 16;
    st_handoff(rs, dst, pack8(o, 0)); st_handoff(rs, dst + 1024, pack8(o, 1));
}

constexpr int L_WA = 0;
constexpr int L_WA_END = 448 * 224;
constexpr int L_MB = 384 * 224, L_ML = L_MB + 65536;
constexpr int L_Q = L_MB, L_K0 = L_Q + 8192, L_M0 = L_K0 + 2048;
constexpr int L_WB = 102400, L_WB_END = L_WB + 448 * 64;
constexpr int L_OA = 102400, L_EX = 147456;

template <int N> DI void ld_handoff(rsrc_t rs, unsigned off, u32x4 (&v)[N]) {
    for (int spin = 0; spin < (1 << 20); ++spin) {
        bool ok = true;
#pragma unroll
        for (int i = 0; i < N; ++i) v[i] = __builtin_amdgcn_raw_buffer_load_b128(rs, off + i * 1024, 0, 16);
#pragma unroll
        for (int i = 0; i < N; ++i) ok = ok && v[i][0] != SENT && v[i][1] != SENT && v[i][2] != SENT && v[i][3] != SENT;
        if (__all(ok)) break;
        __builtin_amdgcn_s_sleep(2);
    }
}

template <bool LAST> DI void attn_block(f32x16& o0, f32x16& o1, f32x16& o2, f32x16& o3, f32x16& sa, const bf16x8 (&q)[8],
                   bf16x8& ka0, bf16x8& ka1, bf16x8& va0, bf16x8& va1, bf16x8& kb0, bf16x8& kb1, bf16x8& vb0, bf16x8& vb1,
                   unsigned voff, unsigned wvoff, const char*& wp, unsigned& wl, const char* ga, const char* gb) {
    unsigned m0save;
    if (LAST) {
    asm volatile(
    "s_mov_b32 s43, m0\n\t"
    "v_mfma_f32_32x32x16_bf16 v[80:95], v[128:131], v[104:107], 0\n\t"
    "v_exp_f32 v64, v64\n\t"
    "v_exp_f32 v65, v65\n\t"
    "v_exp_f32 v66, v66\n\t"
    "v_exp_f32 v67, v67\n\t"
    "v_cvt_pk_bf16_f32 v160, v64, v65\n\t"
    "v_cvt_pk_bf16_f32 v161, v66, v67\n\t"
    "v_mfma_f32_32x32x16_bf16 v[80:95], v[132:135], v[108:111], v[80:95]\n\t"
    "v_exp_f32 v68, v68\n\t"
    "v_exp_f32 v69, v69\n\t"
    "v_exp_f32 v70, v70\n\t"
    "v_exp_f32 v71, v71\n\t"
    "v_cvt_pk_bf16_f32 v162, v68, v69\n\t"
    "v_cvt_pk_bf16_f32 v163, v70, v71\n\t"
    "v_exp_f32 v72, v72\n\t"
    "v_exp_f32 v73, v73\n\t"
    "v_exp_f32 v74, v74\n\t"
    "v_exp_f32 v75, v75\n\t"
    "v_cvt_pk_bf16_f32 v164, v72, v73\n\t"
    "v_cvt_pk_bf16_f32 v165, v74, v75\n\t"
    "v_exp_f32 v76, v76\n\t"
    "v_exp_f32 v77, v77\n\t"
    "v_exp_f32 v78, v78\n\t"
    "v_exp_f32 v79, v79\n\t"
    "v_cvt_pk_bf16_f32 v166, v76, v77\n\t"
    "v_cvt_pk_bf16_f32 v167, v78, v79\n\t"
    "v_mfma_f32_32x32x16_bf16 v[64:79], v[128:131], v[112:115], 0\n\t"
    "v_exp_f32 v80, v80\n\t"
    "v_exp_f32 v81, v81\n\t"
    "v_exp_f32 v82, v82\n\t"
    "v_exp_f32 v83, v83\n\t"
    "v_cvt_pk_bf16_f32 v168, v80, v81\n\t"
    "v_cvt_pk_bf16_f32 v169, v82, v83\n\t"
    "v_mfma_f32_32x32x16_bf16 v[64:79], v[132:135], v[116:119], v[64:79]\n\t"
    "v_exp_f32 v84, v84\n\t"
    "v_exp_f32 v85, v85\n\t"
    "v_exp_f32 v86, v86\n\t"
    "v_exp_f32 v87, v87\n\t"
    "v_cvt_pk_bf16_f32 v170, v84, v85\n\t"
    "v_cvt_pk_bf16_f32 v171, v86, v87\n\t"
    "s_waitcnt vmcnt(6)\n\t"
    "v_mfma_f32_32x32x16_bf16 v[0:15], v[136:139], v[160:163], v[0:15]\n\t"
    "v_exp_f32 v88, v88\n\t"
    "v_exp_f32 v89, v89\n\t"
    "v_exp_f32 v90, v90\n\t"
    "v_exp_f32 v91, v91\n\t"
    "v_cvt_pk_bf16_f32 v172, v88, v89\n\t"
    "v_cvt_pk_bf16_f32 v173, v90, v91\n\t"
    "v_mfma_f32_32x32x16_bf16 v[0:15], v[140:143], v[164:167], v[0:15]\n\t"
    "v_exp_f32 v92, v92\n\t"
    "v_exp_f32 v93, v93\n\t"
    "v_exp_f32 v94, v94\n\t"
    "v_exp_f32 v95, v95\n\t"
    "v_cvt_pk_bf16_f32 v174, v92, v93\n\t"
    "v_cvt_pk_bf16_f32 v175, v94, v95\n\t"
    "v_mfma_f32_32x32x16_bf16 v[80:95], v[128:131], v[120:123], 0\n\t"
    "v_exp_f32 v64, v64\n\t"
    "v_exp_f32 v65, v65\n\t"
    "v_exp_f32 v66, v66\n\t"
    "v_exp_f32 v67, v67\n\t"
    "v_cvt_pk_bf16_f32 v160, v64, v65\n\t"
    "v_cvt_pk_bf16_f32 v161, v66, v67\n\t"
    "v_mfma_f32_32x32x16_bf16 v[80:95], v[132:135], v[124:127], v[80:95]\n\t"
    "v_exp_f32 v68, v68\n\t"
    "v_exp_f32 v69, v69\n\t"
    "v_exp_f32 v70, v70\n\t"
    "v_exp_f32 v71, v71\n\t"
    "v_cvt_pk_bf16_f32 v162, v68, v69\n\t"
    "v_cvt_pk_bf16_f32 v163, v70, v71\n\t"
    "v_mfma_f32_32x32x16_bf16 v[16:31], v[136:139], v[168:171], v[16:31]\n\t"
    "v_exp_f32 v72, v72\n\t"
    "v_exp_f32 v73, v73\n\t"
    "v_exp_f32 v74, v74\n\t"
    "v_exp_f32 v75, v75\n\t"
    "v_cvt_pk_bf16_f32 v164, v72, v73\n\t"
    "v_cvt_pk_bf16_f32 v165, v74, v75\n\t"
    "v_mfma_f32_32x32x16_bf16 v[16:31], v[140:143], v[172:175], v[16:31]\n\t"
    "v_exp_f32 v76, v76\n\t"
    "v_exp_f32 v77, v77\n\t"
    "v_exp_f32 v78, v78\n\t"
    "v_exp_f32 v79, v79\n\t"
    "v_cvt_pk_bf16_f32 v166, v76, v77\n\t"
    "v_cvt_pk_bf16_f32 v167, v78, v79\n\t"
    "s_mov_b32 m0, s42\n\t"
    "s_add_u32 s42, s42, 0x1c00\n\t"
    "global_load_lds_dwordx4 v177, s[40:41]\n\t"
    "s_add_u32 s40, s40, 0x7000\n\t"
    "s_addc_u32 s41, s41, 0\n\t"
    "s_waitcnt vmcnt(4)\n\t"
    "v_mfma_f32_32x32x16_bf16 v[64:79], v[144:147], v[96:99], 0\n\t"
    "v_exp_f32 v80, v80\n\t"
    "v_exp_f32 v81, v81\n\t"
    "v_exp_f32 v82, v82\n\t"
    "v_exp_f32 v83, v83\n\t"
    "v_cvt_pk_bf16_f32 v168, v80, v81\n\t"
    "v_cvt_pk_bf16_f32 v169, v82, v83\n\t"
    "v_mfma_f32_32x32x16_bf16 v[64:79], v[148:151], v[100:103], v[64:79]\n\t"
    "v_exp_f32 v84, v84\n\t"
    "v_exp_f32 v85, v85\n\t"
    "v_exp_f32 v86, v86\n\t"
    "v_exp_f32 v87, v87\n\t"
    "v_cvt_pk_bf16_f32 v170, v84, v85\n\t"
    "v_cvt_pk_bf16_f32 v171, v86, v87\n\t"
    "v_mfma_f32_32x32x16_bf16 v[32:47], v[136:139], v[160:163], v[32:47]\n\t"
    "v_exp_f32 v88, v88\n\t"
    "v_exp_f32 v89, v89\n\t"
    "v_exp_f32 v90, v90\n\t"
    "v_exp_f32 v91, v91\n\t"
    "v_cvt_pk_bf16_f32 v172, v88, v89\n\t"
    "v_cvt_pk_bf16_f32 v173, v90, v91\n\t"
    "v_mfma_f32_32x32x16_bf16 v[32:47], v[140:143], v[164:167], v[32:47]\n\t"
    "v_exp_f32 v92, v92\n\t"
    "v_exp_f32 v93, v93\n\t"
    "v_exp_f32 v94, v94\n\t"
    "v_exp_f32 v95, v95\n\t"
    "v_cvt_pk_bf16_f32 v174, v92, v93\n\t"
    "v_cvt_pk_bf16_f32 v175, v94, v95\n\t"
    "v_mfma_f32_32x32x16_bf16 v[80:95], v[144:147], v[104:107], 0\n\t"
    "v_exp_f32 v64, v64\n\t"
    "v_exp_f32 v65, v65\n\t"
    "v_exp_f32 v66, v66\n\t"
    "v_exp_f32 v67, v67\n\t"
    "v_cvt_pk_bf16_f32 v160, v64, v65\n\t"
    "v_cvt_pk_bf16_f32 v161, v66, v67\n\t"
    "v_mfma_f32_32x32x16_bf16 v[80:95], v[148:151], v[108:111], v[80:95]\n\t"
    "v_exp_f32 v68, v68\n\t"
    "v_exp_f32 v69, v69\n\t"
    "v_exp_f32 v70, v70\n\t"
    "v_exp_f32 v71, v71\n\t"
    "v_cvt_pk_bf16_f32 v162, v68, v69\n\t"
    "v_cvt_pk_bf16_f32 v163, v70, v71\n\t"
    "v_mfma_f32_32x32x16_bf16 v[48:63], v[136:139], v[168:171], v[48:63]\n\t"
    "v_exp_f32 v72, v72\n\t"
    "v_exp_f32 v73, v73\n\t"
    "v_exp_f32 v74, v74\n\t"
    "v_exp_f32 v75, v75\n\t"
    "v_cvt_pk_bf16_f32 v164, v72, v73\n\t"
    "v_cvt_pk_bf16_f32 v165, v74, v75\n\t"
    "v_mfma_f32_32x32x16_bf16 v[48:63], v[140:143], v[172:175], v[48:63]\n\t"
    "v_exp_f32 v76, v76\n\t"
    "v_exp_f32 v77, v77\n\t"
    "v_exp_f32 v78, v78\n\t"
    "v_exp_f32 v79, v79\n\t"
    "v_cvt_pk_bf16_f32 v166, v76, v77\n\t"
    "v_cvt_pk_bf16_f32 v167, v78, v79\n\t"
    "s_mov_b32 m0, s42\n\t"
    "s_add_u32 s42, s42, 0x1c00\n\t"
    "global_load_lds_dwordx4 v177, s[40:41]\n\t"
    "s_add_u32 s40, s40, 0x7000\n\t"
    "s_addc_u32 s41, s41, 0\n\t"
    "v_mfma_f32_32x32x16_bf16 v[64:79], v[144:147], v[112:115], 0\n\t"
    "v_exp_f32 v80, v80\n\t"
    "v_exp_f32 v81, v81\n\t"
    "v_exp_f32 v82, v82\n\t"
    "v_exp_f32 v83, v83\n\t"
    "v_cvt_pk_bf16_f32 v168, v80, v81\n\t"
    "v_cvt_pk_bf16_f32 v169, v82, v83\n\t"
    "v_mfma_f32_32x32x16_bf16 v[64:79], v[148:151], v[116:119], v[64:79]\n\t"
    "v_exp_f32 v84, v84\n\t"
    "v_exp_f32 v85, v85\n\t"
    "v_exp_f32 v86, v86\n\t"
    "v_exp_f32 v87, v87\n\t"
    "v_cvt_pk_bf16_f32 v170, v84, v85\n\t"
    "v_cvt_pk_bf16_f32 v171, v86, v87\n\t"
    "s_waitcnt vmcnt(2)\n\t"
    "v_mfma_f32_32x32x16_bf16 v[0:15], v[152:155], v[160:163], v[0:15]\n\t"
    "v_exp_f32 v88, v88\n\t"
    "v_exp_f32 v89, v89\n\t"
    "v_exp_f32 v90, v90\n\t"
    "v_exp_f32 v91, v91\n\t"
    "v_cvt_pk_bf16_f32 v172, v88, v89\n\t"
    "v_cvt_pk_bf16_f32 v173, v90, v91\n\t"
    "v_mfma_f32_32x32x16_bf16 v[0:15], v[156:159], v[164:167], v[0:15]\n\t"
    "v_exp_f32 v92, v92\n\t"
    "v_exp_f32 v93, v93\n\t"
    "v_exp_f32 v94, v94\n\t"
    "v_exp_f32 v95, v95\n\t"
    "v_cvt_pk_bf16_f32 v174, v92, v93\n\t"
    "v_cvt_pk_bf16_f32 v175, v94, v95\n\t"
    "v_mfma_f32_32x32x16_bf16 v[80:95], v[144:147], v[120:123], 0\n\t"
    "v_exp_f32 v64, v64\n\t"
    "v_exp_f32 v65, v65\n\t"
    "v_exp_f32 v66, v66\n\t"
    "v_exp_f32 v67, v67\n\t"
    "v_cvt_pk_bf16_f32 v160, v64, v65\n\t"
    "v_cvt_pk_bf16_f32 v161, v66, v67\n\t"
    "v_mfma_f32_32x32x16_bf16 v[80:95], v[148:151], v[124:127], v[80:95]\n\t"
    "v_exp_f32 v68, v68\n\t"
    "v_exp_f32 v69, v69\n\t"
    "v_exp_f32 v70, v70\n\t"
    "v_exp_f32 v71, v71\n\t"
    "v_cvt_pk_bf16_f32 v162, v68, v69\n\t"
    "v_cvt_pk_bf16_f32 v163, v70, v71\n\t"
    "v_mfma_f32_32x32x16_bf16 v[16:31], v[152:155], v[168:171], v[16:31]\n\t"
    "v_exp_f32 v72, v72\n\t"
    "v_exp_f32 v73, v73\n\t"
    "v_exp_f32 v74, v74\n\t"
    "v_exp_f32 v75, v75\n\t"
    "v_cvt_pk_bf16_f32 v164, v72, v73\n\t"
    "v_cvt_pk_bf16_f32 v165, v74, v75\n\t"
    "v_mfma_f32_32x32x16_bf16 v[16:31], v[156:159], v[172:175], v[16:31]\n\t"
    "v_exp_f32 v76, v76\n\t"
    "v_exp_f32 v77, v77\n\t"
    "v_exp_f32 v78, v78\n\t"
    "v_exp_f32 v79, v79\n\t"
    "v_cvt_pk_bf16_f32 v166, v76, v77\n\t"
    "v_cvt_pk_bf16_f32 v167, v78, v79\n\t"
    "s_mov_b32 m0, s42\n\t"
    "s_add_u32 s42, s42, 0x1c00\n\t"
    "global_load_lds_dwordx4 v177, s[40:41]\n\t"
    "s_add_u32 s40, s40, 0x7000\n\t"
    "s_addc_u32 s41, s41, 0\n\t"
    "v_exp_f32 v80, v80\n\t"
    "v_exp_f32 v81, v81\n\t"
    "v_exp_f32 v82, v82\n\t"
    "v_exp_f32 v83, v83\n\t"
    "v_cvt_pk_bf16_f32 v168, v80, v81\n\t"
    "v_cvt_pk_bf16_f32 v169, v82, v83\n\t"
    "v_exp_f32 v84, v84\n\t"
    "v_exp_f32 v85, v85\n\t"
    "v_exp_f32 v86, v86\n\t"
    "v_exp_f32 v87, v87\n\t"
    "v_cvt_pk_bf16_f32 v170, v84, v85\n\t"
    "v_cvt_pk_bf16_f32 v171, v86, v87\n\t"
    "v_mfma_f32_32x32x16_bf16 v[32:47], v[152:155], v[160:163], v[32:47]\n\t"
    "v_exp_f32 v88, v88\n\t"
    "v_exp_f32 v89, v89\n\t"
    "v_exp_f32 v90, v90\n\t"
    "v_exp_f32 v91, v91\n\t"
    "v_cvt_pk_bf16_f32 v172, v88, v89\n\t"
    "v_cvt_pk_bf16_f32 v173, v90, v91\n\t"
    "v_mfma_f32_32x32x16_bf16 v[32:47], v[156:159], v[164:167], v[32:47]\n\t"
    "v_exp_f32 v92, v92\n\t"
    "v_exp_f32 v93, v93\n\t"
    "v_exp_f32 v94, v94\n\t"
    "v_exp_f32 v95, v95\n\t"
    "v_cvt_pk_bf16_f32 v174, v92, v93\n\t"
    "v_cvt_pk_bf16_f32 v175, v94, v95\n\t"
    "v_mfma_f32_32x32x16_bf16 v[48:63], v[152:155], v[168:171], v[48:63]\n\t"
    "s_nop 1\n\t"
    "v_mfma_f32_32x32x16_bf16 v[48:63], v[156:159], v[172:175], v[48:63]\n\t"
    "s_mov_b32 m0, s43\n\t"
    "s_nop 7\n\t"
    "s_nop 3"
        : "+{v[0:15]}"(o0), "+{v[16:31]}"(o1), "+{v[32:47]}"(o2), "+{v[48:63]}"(o3), "+{v[64:79]}"(sa),
          "+{v[128:131]}"(ka0), "+{v[132:135]}"(ka1), "+{v[136:139]}"(va0), "+{v[140:143]}"(va1),
          "+{v[144:147]}"(kb0), "+{v[148:151]}"(kb1), "+{v[152:155]}"(vb0), "+{v[156:159]}"(vb1),
          "+{s[40:41]}"(wp), "+{s42}"(wl), "={s43}"(m0save)
        : "{v[96:99]}"(q[0]), "{v[100:103]}"(q[1]), "{v[104:107]}"(q[2]), "{v[108:111]}"(q[3]),
          "{v[112:115]}"(q[4]), "{v[116:119]}"(q[5]), "{v[120:123]}"(q[6]), "{v[124:127]}"(q[7]),
          "{v176}"(voff), "{v177}"(wvoff), "{s[44:45]}"(ga), "{s[46:47]}"(gb)
        : "memory", "scc",
          "v80", "v81", "v82", "v83", "v84", "v85", "v86", "v87", "v88", "v89", "v90", "v91", "v92", "v93", "v94", "v95",
          "v160", "v161", "v162", "v163", "v164", "v165", "v166", "v167", "v168", "v169", "v170", "v171", "v172", "v173", "v174", "v175");
    } else {
    asm volatile(
    "s_mov_b32 s43, m0\n\t"
    "v_mfma_f32_32x32x16_bf16 v[80:95], v[128:131], v[104:107], 0\n\t"
    "v_exp_f32 v64, v64\n\t"
    "v_exp_f32 v65, v65\n\t"
    "v_exp_f32 v66, v66\n\t"
    "v_exp_f32 v67, v67\n\t"
    "v_cvt_pk_bf16_f32 v160, v64, v65\n\t"
    "v_cvt_pk_bf16_f32 v161, v66, v67\n\t"
    "v_mfma_f32_32x32x16_bf16 v[80:95], v[132:135], v[108:111], v[80:95]\n\t"
    "v_exp_f32 v68, v68\n\t"
    "v_exp_f32 v69, v69\n\t"
    "v_exp_f32 v70, v70\n\t"
    "v_exp_f32 v71, v71\n\t"
    "v_cvt_pk_bf16_f32 v162, v68, v69\n\t"
    "v_cvt_pk_bf16_f32 v163, v70, v71\n\t"
    "v_exp_f32 v72, v72\n\t"
    "v_exp_f32 v73, v73\n\t"
    "v_exp_f32 v74, v74\n\t"
    "v_exp_f32 v75, v75\n\t"
    "v_cvt_pk_bf16_f32 v164, v72, v73\n\t"
    "v_cvt_pk_bf16_f32 v165, v74, v75\n\t"
    "v_exp_f32 v76, v76\n\t"
    "v_exp_f32 v77, v77\n\t"
    "v_exp_f32 v78, v78\n\t"
    "v_exp_f32 v79, v79\n\t"
    "v_cvt_pk_bf16_f32 v166, v76, v77\n\t"
    "v_cvt_pk_bf16_f32 v167, v78, v79\n\t"
    "v_mfma_f32_32x32x16_bf16 v[64:79], v[128:131], v[112:115], 0\n\t"
    "v_exp_f32 v80, v80\n\t"
    "v_exp_f32 v81, v81\n\t"
    "v_exp_f32 v82, v82\n\t"
    "v_exp_f32 v83, v83\n\t"
    "v_cvt_pk_bf16_f32 v168, v80, v81\n\t"
    "v_cvt_pk_bf16_f32 v169, v82, v83\n\t"
    "v_mfma_f32_32x32x16_bf16 v[64:79], v[132:135], v[116:119], v[64:79]\n\t"
    "v_exp_f32 v84, v84\n\t"
    "v_exp_f32 v85, v85\n\t"
    "v_exp_f32 v86, v86\n\t"
    "v_exp_f32 v87, v87\n\t"
    "v_cvt_pk_bf16_f32 v170, v84, v85\n\t"
    "v_cvt_pk_bf16_f32 v171, v86, v87\n\t"
    "s_waitcnt vmcnt(6)\n\t"
    "v_mfma_f32_32x32x16_bf16 v[0:15], v[136:139], v[160:163], v[0:15]\n\t"
    "v_exp_f32 v88, v88\n\t"
    "v_exp_f32 v89, v89\n\t"
    "v_exp_f32 v90, v90\n\t"
    "v_exp_f32 v91, v91\n\t"
    "v_cvt_pk_bf16_f32 v172, v88, v89\n\t"
    "v_cvt_pk_bf16_f32 v173, v90, v91\n\t"
    "v_mfma_f32_32x32x16_bf16 v[0:15], v[140:143], v[164:167], v[0:15]\n\t"
    "v_exp_f32 v92, v92\n\t"
    "v_exp_f32 v93, v93\n\t"
    "v_exp_f32 v94, v94\n\t"
    "v_exp_f32 v95, v95\n\t"
    "v_cvt_pk_bf16_f32 v174, v92, v93\n\t"
    "v_cvt_pk_bf16_f32 v175, v94, v95\n\t"
    "v_mfma_f32_32x32x16_bf16 v[80:95], v[128:131], v[120:123], 0\n\t"
    "v_exp_f32 v64, v64\n\t"
    "v_exp_f32 v65, v65\n\t"
    "v_exp_f32 v66, v66\n\t"
    "v_exp_f32 v67, v67\n\t"
    "v_cvt_pk_bf16_f32 v160, v64, v65\n\t"
    "v_cvt_pk_bf16_f32 v161, v66, v67\n\t"
    "v_mfma_f32_32x32x16_bf16 v[80:95], v[132:135], v[124:127], v[80:95]\n\t"
    "v_exp_f32 v68, v68\n\t"
    "v_exp_f32 v69, v69\n\t"
    "v_exp_f32 v70, v70\n\t"
    "v_exp_f32 v71, v71\n\t"
    "v_cvt_pk_bf16_f32 v162, v68, v69\n\t"
    "v_cvt_pk_bf16_f32 v163, v70, v71\n\t"
    "v_mfma_f32_32x32x16_bf16 v[16:31], v[136:139], v[168:171], v[16:31]\n\t"
    "v_exp_f32 v72, v72\n\t"
    "v_exp_f32 v73, v73\n\t"
    "v_exp_f32 v74, v74\n\t"
    "v_exp_f32 v75, v75\n\t"
    "v_cvt_pk_bf16_f32 v164, v72, v73\n\t"
    "v_cvt_pk_bf16_f32 v165, v74, v75\n\t"
    "v_mfma_f32_32x32x16_bf16 v[16:31], v[140:143], v[172:175], v[16:31]\n\t"
    "v_exp_f32 v76, v76\n\t"
    "v_exp_f32 v77, v77\n\t"
    "v_exp_f32 v78, v78\n\t"
    "v_exp_f32 v79, v79\n\t"
    "v_cvt_pk_bf16_f32 v166, v76, v77\n\t"
    "v_cvt_pk_bf16_f32 v167, v78, v79\n\t"
    "global_load_dwordx4 v[128:131], v176, s[44:45] offset:0\n\t"
    "global_load_dwordx4 v[132:135], v176, s[44:45] offset:1024\n\t"
    "s_mov_b32 m0, s42\n\t"
    "s_add_u32 s42, s42, 0x1c00\n\t"
    "global_load_lds_dwordx4 v177, s[40:41]\n\t"
    "s_add_u32 s40, s40, 0x7000\n\t"
    "s_addc_u32 s41, s41, 0\n\t"
    "s_waitcnt vmcnt(6)\n\t"
    "v_mfma_f32_32x32x16_bf16 v[64:79], v[144:147], v[96:99], 0\n\t"
    "v_exp_f32 v80, v80\n\t"
    "v_exp_f32 v81, v81\n\t"
    "v_exp_f32 v82, v82\n\t"
    "v_exp_f32 v83, v83\n\t"
    "v_cvt_pk_bf16_f32 v168, v80, v81\n\t"
    "v_cvt_pk_bf16_f32 v169, v82, v83\n\t"
    "v_mfma_f32_32x32x16_bf16 v[64:79], v[148:151], v[100:103], v[64:79]\n\t"
    "v_exp_f32 v84, v84\n\t"
    "v_exp_f32 v85, v85\n\t"
    "v_exp_f32 v86, v86\n\t"
    "v_exp_f32 v87, v87\n\t"
    "v_cvt_pk_bf16_f32 v170, v84, v85\n\t"
    "v_cvt_pk_bf16_f32 v171, v86, v87\n\t"
    "v_mfma_f32_32x32x16_bf16 v[32:47], v[136:139], v[160:163], v[32:47]\n\t"
    "v_exp_f32 v88, v88\n\t"
    "v_exp_f32 v89, v89\n\t"
    "v_exp_f32 v90, v90\n\t"
    "v_exp_f32 v91, v91\n\t"
    "v_cvt_pk_bf16_f32 v172, v88, v89\n\t"
    "v_cvt_pk_bf16_f32 v173, v90, v91\n\t"
    "v_mfma_f32_32x32x16_bf16 v[32:47], v[140:143], v[164:167], v[32:47]\n\t"
    "v_exp_f32 v92, v92\n\t"
    "v_exp_f32 v93, v93\n\t"
    "v_exp_f32 v94, v94\n\t"
    "v_exp_f32 v95, v95\n\t"
    "v_cvt_pk_bf16_f32 v174, v92, v93\n\t"
    "v_cvt_pk_bf16_f32 v175, v94, v95\n\t"
    "v_mfma_f32_32x32x16_bf16 v[80:95], v[144:147], v[104:107], 0\n\t"
    "v_exp_f32 v64, v64\n\t"
    "v_exp_f32 v65, v65\n\t"
    "v_exp_f32 v66, v66\n\t"
    "v_exp_f32 v67, v67\n\t"
    "v_cvt_pk_bf16_f32 v160, v64, v65\n\t"
    "v_cvt_pk_bf16_f32 v161, v66, v67\n\t"
    "v_mfma_f32_32x32x16_bf16 v[80:95], v[148:151], v[108:111], v[80:95]\n\t"
    "v_exp_f32 v68, v68\n\t"
    "v_exp_f32 v69, v69\n\t"
    "v_exp_f32 v70, v70\n\t"
    "v_exp_f32 v71, v71\n\t"
    "v_cvt_pk_bf16_f32 v162, v68, v69\n\t"
    "v_cvt_pk_bf16_f32 v163, v70, v71\n\t"
    "v_mfma_f32_32x32x16_bf16 v[48:63], v[136:139], v[168:171], v[48:63]\n\t"
    "v_exp_f32 v72, v72\n\t"
    "v_exp_f32 v73, v73\n\t"
    "v_exp_f32 v74, v74\n\t"
    "v_exp_f32 v75, v75\n\t"
    "v_cvt_pk_bf16_f32 v164, v72, v73\n\t"
    "v_cvt_pk_bf16_f32 v165, v74, v75\n\t"
    "v_mfma_f32_32x32x16_bf16 v[48:63], v[140:143], v[172:175], v[48:63]\n\t"
    "v_exp_f32 v76, v76\n\t"
    "v_exp_f32 v77, v77\n\t"
    "v_exp_f32 v78, v78\n\t"
    "v_exp_f32 v79, v79\n\t"
    "v_cvt_pk_bf16_f32 v166, v76, v77\n\t"
    "v_cvt_pk_bf16_f32 v167, v78, v79\n\t"
    "global_load_dwordx4 v[136:139], v176, s[44:45] offset:2048\n\t"
    "global_load_dwordx4 v[140:143], v176, s[44:45] offset:3072\n\t"
    "s_mov_b32 m0, s42\n\t"
    "s_add_u32 s42, s42, 0x1c00\n\t"
    "global_load_lds_dwordx4 v177, s[40:41]\n\t"
    "s_add_u32 s40, s40, 0x7000\n\t"
    "s_addc_u32 s41, s41, 0\n\t"
    "v_mfma_f32_32x32x16_bf16 v[64:79], v[144:147], v[112:115], 0\n\t"
    "v_exp_f32 v80, v80\n\t"
    "v_exp_f32 v81, v81\n\t"
    "v_exp_f32 v82, v82\n\t"
    "v_exp_f32 v83, v83\n\t"
    "v_cvt_pk_bf16_f32 v168, v80, v81\n\t"
    "v_cvt_pk_bf16_f32 v169, v82, v83\n\t"
    "v_mfma_f32_32x32x16_bf16 v[64:79], v[148:151], v[116:119], v[64:79]\n\t"
    "v_exp_f32 v84, v84\n\t"
    "v_exp_f32 v85, v85\n\t"
    "v_exp_f32 v86, v86\n\t"
    "v_exp_f32 v87, v87\n\t"
    "v_cvt_pk_bf16_f32 v170, v84, v85\n\t"
    "v_cvt_pk_bf16_f32 v171, v86, v87\n\t"
    "s_waitcnt vmcnt(6)\n\t"
    "v_mfma_f32_32x32x16_bf16 v[0:15], v[152:155], v[160:163], v[0:15]\n\t"
    "v_exp_f32 v88, v88\n\t"
    "v_exp_f32 v89, v89\n\t"
    "v_exp_f32 v90, v90\n\t"
    "v_exp_f32 v91, v91\n\t"
    "v_cvt_pk_bf16_f32 v172, v88, v89\n\t"
    "v_cvt_pk_bf16_f32 v173, v90, v91\n\t"
    "v_mfma_f32_32x32x16_bf16 v[0:15], v[156:159], v[164:167], v[0:15]\n\t"
    "v_exp_f32 v92, v92\n\t"
    "v_exp_f32 v93, v93\n\t"
    "v_exp_f32 v94, v94\n\t"
    "v_exp_f32 v95, v95\n\t"
    "v_cvt_pk_bf16_f32 v174, v92, v93\n\t"
    "v_cvt_pk_bf16_f32 v175, v94, v95\n\t"
    "v_mfma_f32_32x32x16_bf16 v[80:95], v[144:147], v[120:123], 0\n\t"
    "v_exp_f32 v64, v64\n\t"
    "v_exp_f32 v65, v65\n\t"
    "v_exp_f32 v66, v66\n\t"
    "v_exp_f32 v67, v67\n\t"
    "v_cvt_pk_bf16_f32 v160, v64, v65\n\t"
    "v_cvt_pk_bf16_f32 v161, v66, v67\n\t"
    "v_mfma_f32_32x32x16_bf16 v[80:95], v[148:151], v[124:127], v[80:95]\n\t"
    "v_exp_f32 v68, v68\n\t"
    "v_exp_f32 v69, v69\n\t"
    "v_exp_f32 v70, v70\n\t"
    "v_exp_f32 v71, v71\n\t"
    "v_cvt_pk_bf16_f32 v162, v68, v69\n\t"
    "v_cvt_pk_bf16_f32 v163, v70, v71\n\t"
    "v_mfma_f32_32x32x16_bf16 v[16:31], v[152:155], v[168:171], v[16:31]\n\t"
    "v_exp_f32 v72, v72\n\t"
    "v_exp_f32 v73, v73\n\t"
    "v_exp_f32 v74, v74\n\t"
    "v_exp_f32 v75, v75\n\t"
    "v_cvt_pk_bf16_f32 v164, v72, v73\n\t"
    "v_cvt_pk_bf16_f32 v165, v74, v75\n\t"
    "v_mfma_f32_32x32x16_bf16 v[16:31], v[156:159], v[172:175], v[16:31]\n\t"
    "v_exp_f32 v76, v76\n\t"
    "v_exp_f32 v77, v77\n\t"
    "v_exp_f32 v78, v78\n\t"
    "v_exp_f32 v79, v79\n\t"
    "v_cvt_pk_bf16_f32 v166, v76, v77\n\t"
    "v_cvt_pk_bf16_f32 v167, v78, v79\n\t"
    "global_load_dwordx4 v[144:147], v176, s[46:47] offset:0\n\t"
    "global_load_dwordx4 v[148:151], v176, s[46:47] offset:1024\n\t"
    "s_mov_b32 m0, s42\n\t"
    "s_add_u32 s42, s42, 0x1c00\n\t"
    "global_load_lds_dwordx4 v177, s[40:41]\n\t"
    "s_add_u32 s40, s40, 0x7000\n\t"
    "s_addc_u32 s41, s41, 0\n\t"
    "s_waitcnt vmcnt(7)\n\t"
    "v_mfma_f32_32x32x16_bf16 v[64:79], v[128:131], v[96:99], 0\n\t"
    "v_exp_f32 v80, v80\n\t"
    "v_exp_f32 v81, v81\n\t"
    "v_exp_f32 v82, v82\n\t"
    "v_exp_f32 v83, v83\n\t"
    "v_cvt_pk_bf16_f32 v168, v80, v81\n\t"
    "v_cvt_pk_bf16_f32 v169, v82, v83\n\t"
    "v_mfma_f32_32x32x16_bf16 v[64:79], v[132:135], v[100:103], v[64:79]\n\t"
    "v_exp_f32 v84, v84\n\t"
    "v_exp_f32 v85, v85\n\t"
    "v_exp_f32 v86, v86\n\t"
    "v_exp_f32 v87, v87\n\t"
    "v_cvt_pk_bf16_f32 v170, v84, v85\n\t"
    "v_cvt_pk_bf16_f32 v171, v86, v87\n\t"
    "v_mfma_f32_32x32x16_bf16 v[32:47], v[152:155], v[160:163], v[32:47]\n\t"
    "v_exp_f32 v88, v88\n\t"
    "v_exp_f32 v89, v89\n\t"
    "v_exp_f32 v90, v90\n\t"
    "v_exp_f32 v91, v91\n\t"
    "v_cvt_pk_bf16_f32 v172, v88, v89\n\t"
    "v_cvt_pk_bf16_f32 v173, v90, v91\n\t"
    "v_mfma_f32_32x32x16_bf16 v[32:47], v[156:159], v[164:167], v[32:47]\n\t"
    "v_exp_f32 v92, v92\n\t"
    "v_exp_f32 v93, v93\n\t"
    "v_exp_f32 v94, v94\n\t"
    "v_exp_f32 v95, v95\n\t"
    "v_cvt_pk_bf16_f32 v174, v92, v93\n\t"
    "v_cvt_pk_bf16_f32 v175, v94, v95\n\t"
    "v_mfma_f32_32x32x16_bf16 v[48:63], v[152:155], v[168:171], v[48:63]\n\t"
    "s_nop 1\n\t"
    "v_mfma_f32_32x32x16_bf16 v[48:63], v[156:159], v[172:175], v[48:63]\n\t"
    "global_load_dwordx4 v[152:155], v176, s[46:47] offset:2048\n\t"
    "global_load_dwordx4 v[156:159], v176, s[46:47] offset:3072\n\t"
    "s_mov_b32 m0, s43\n\t"
    "s_nop 7\n\t"
    "s_nop 3"
        : "+{v[0:15]}"(o0), "+{v[16:31]}"(o1), "+{v[32:47]}"(o2), "+{v[48:63]}"(o3), "+{v[64:79]}"(sa),
          "+{v[128:131]}"(ka0), "+{v[132:135]}"(ka1), "+{v[136:139]}"(va0), "+{v[140:143]}"(va1),
          "+{v[144:147]}"(kb0), "+{v[148:151]}"(kb1), "+{v[152:155]}"(vb0), "+{v[156:159]}"(vb1),
          "+{s[40:41]}"(wp), "+{s42}"(wl), "={s43}"(m0save)
        : "{v[96:99]}"(q[0]), "{v[100:103]}"(q[1]), "{v[104:107]}"(q[2]), "{v[108:111]}"(q[3]),
          "{v[112:115]}"(q[4]), "{v[116:119]}"(q[5]), "{v[120:123]}"(q[6]), "{v[124:127]}"(q[7]),
          "{v176}"(voff), "{v177}"(wvoff), "{s[44:45]}"(ga), "{s[46:47]}"(gb)
        : "memory", "scc",
          "v80", "v81", "v82", "v83", "v84", "v85", "v86", "v87", "v88", "v89", "v90", "v91", "v92", "v93", "v94", "v95",
          "v160", "v161", "v162", "v163", "v164", "v165", "v166", "v167", "v168", "v169", "v170", "v171", "v172", "v173", "v174", "v175");
    }
}
DI void rescale(f32x16& o, float& mref, bf16x8& q1, int hh, float& dout) {
    const float l = swap_hi(o[12]);
    const float mn = l > 65536.f ? mref + __builtin_amdgcn_logf(l) : mref;
    const unsigned mb = pk(-mn, 0.f) & 0xffffu; const float m2 = -__uint_as_float(mb << 16), d = m2 - mref, f = __builtin_amdgcn_exp2f(-d);
    mref = m2; if (hh) q1[4] = (short)mb;
#pragma unroll
    for (int i = 0; i < 16; ++i) o[i] *= f;
    dout = d;
}
DI void attn_safe(const char* kv, int lane, bf16x8 q0, bf16x8 q1, f32x16& o, float& mref) {
    const int hh = lane >> 5; const f32x16 zero = {};
    if (hh) q1[4] = 0;
    float m = -INFINITY; o = zero;
    for (int tt = 0; tt < 8; ++tt) {
        const char* kt = kv + (size_t)tt * 4096 + lane * 16;
        f32x16 x = MFMA(*(const bf16x8*)kt, q0, zero); x = MFMA(*(const bf16x8*)(kt + 1024), q1, x);
        const float mn = __builtin_fmaxf(m, halfmax(max16(x))), f = __builtin_amdgcn_exp2f(m - mn);
#pragma unroll
        for (int i = 0; i < 16; ++i) { x[i] = __builtin_amdgcn_exp2f(x[i] - mn); o[i] *= f; }
        m = mn;
        o = MFMA(*(const bf16x8*)(kt + 2048), pack8(x, 0), o); o = MFMA(*(const bf16x8*)(kt + 3072), pack8(x, 1), o);
    }
    mref = m;
}
DI bool o_bad(const f32x16& o, int hh) {
    float x = 0.f, y = 0.f;
#pragma unroll
    for (int i = 0; i < 16; i += 2) { x = __builtin_fmaf(o[i], 0.f, x); y = __builtin_fmaf(o[i + 1], 0.f, y); }
    const float l = o[12];
    return !(x + y == 0.f) || (hh && !(l > 7.5e-37f && l < 1.3e36f));
}

__global__ __launch_bounds__(512, 2) void k_attn(Params p) {
    extern __shared__ __attribute__((aligned(16))) char ldsg[];
    const int tid = threadIdx.x, lane = tid & 63, w = __builtin_amdgcn_readfirstlane(tid >> 6), b = blockIdx.x;
    ST_DECL; STAMP(0); RSTAMP(22);
    const int xcd = b & 7, j = b >> 3, h = 2 * xcd + (j >> 4), qb = j & 15;
    const int r = lane & 31, hh = lane >> 5;
    lptr lds = (lptr)ldsg; const unsigned lds0 = (unsigned)(uintptr_t)ldsg;
    const rsrc_t rs = ws_rsrc(p.ws);
    const unsigned vo = lane * 16;
    const char* kvh = p.ws + WS_KV + (size_t)(h * 64) * 4096;
    const char* kvw = kvh + (size_t)w * 8 * 4096;
    glds(p.ws + WS_Q + (size_t)((h * 64 + qb * 4) * 2 + w) * 1024, vo, lds0 + L_Q + w * 1024);
    if (w < 2) glds(kvh + w * 1024, vo, lds0 + L_K0 + w * 1024);
    bf16x8 ka0 = *(const bf16x8*)(kvw + vo), ka1 = *(const bf16x8*)(kvw + 1024 + vo), va0 = *(const bf16x8*)(kvw + 2048 + vo), va1 = *(const bf16x8*)(kvw + 3072 + vo);
    bf16x8 kb0 = *(const bf16x8*)(kvw + 4096 + vo), kb1 = *(const bf16x8*)(kvw + 5120 + vo), vb0 = *(const bf16x8*)(kvw + 6144 + vo), vb1 = *(const bf16x8*)(kvw + 7168 + vo);
    if (w < 2) wait_vm<8>(); else wait_vm<8>();
    bar();
    STAMP(1);
    const f32x16 zero = {};
    if (w < 4) {
        f32x16 s = MFMA(lds16(lds + L_K0 + vo), lds16(lds + L_Q + w * 2048 + vo), zero); s = MFMA(lds16(lds + L_K0 + 1024 + vo), lds16(lds + L_Q + w * 2048 + 1024 + vo), s);
        const float m = halfmax(max16(s));
        if (!hh) *(LAS unsigned*)(lds + L_M0 + w * 128 + r * 4) = pk(-m, 0.f) & 0xffffu;
    }
    bf16x8 q[8];
#pragma unroll
    for (int i = 0; i < 8; ++i) q[i] = lds16(lds + L_Q + i * 1024 + vo);
    bar();
    float mr0, mr1, mr2, mr3;
    { const unsigned b0 = *(LAS unsigned*)(lds + L_M0 + r * 4), b1 = *(LAS unsigned*)(lds + L_M0 + 128 + r * 4), b2 = *(LAS unsigned*)(lds + L_M0 + 256 + r * 4), b3 = *(LAS unsigned*)(lds + L_M0 + 384 + r * 4);
      mr0 = -__uint_as_float(b0 << 16); mr1 = -__uint_as_float(b1 << 16); mr2 = -__uint_as_float(b2 << 16); mr3 = -__uint_as_float(b3 << 16);
      if (hh) { q[1][4] = (short)b0; q[3][4] = (short)b1; q[5][4] = (short)b2; q[7][4] = (short)b3; } }
    bar();
    STAMP(2);
    __builtin_amdgcn_s_setprio(3);
    if (w < 4) local_unit(p, rs, b >> 2, (b & 3) * 4 + (w & 3), lane);
    STAMP(3);
    f32x16 sa = MFMA(ka0, q[0], zero); sa = MFMA(ka1, q[1], sa);
    f32x16 o0 = {}, o1 = {}, o2 = {}, o3 = {};
    const int orb = qb * 4 + (h >> 2), ocg = h & 3;
    unsigned wvoff;
    { const int bb = w < 7 ? w : 0, n = bb * 64 + lane, rho = n / 14, c = n - rho * 14, k = (rho & ~7) + ((rho & 1) << 2) + ((rho & 7) >> 1); wvoff = k * XROW + c * 16; }
    const char* wp = p.woa + ocg * 224; unsigned wl = lds0 + L_WA + (w < 7 ? w : 0) * 1024;
#define RESCALE_CHECK() do { if (__any(__builtin_fmaxf(__builtin_fmaxf(__builtin_fabsf(o0[12]), __builtin_fabsf(o1[12])), __builtin_fmaxf(__builtin_fabsf(o2[12]), __builtin_fabsf(o3[12]))) > 1.2e24f)) { \
            float d, dd; rescale(o0, mr0, q[1], hh, d); rescale(o1, mr1, q[3], hh, dd); rescale(o2, mr2, q[5], hh, dd); rescale(o3, mr3, q[7], hh, dd); \
            _Pragma("unroll") for (int i = 0; i < 16; ++i) sa[i] -= d; } } while (0)
    __builtin_amdgcn_s_setprio(3);
#pragma unroll 1
    for (int it = 0; it < 3; ++it) {
        const char* ga = kvw + (size_t)(2 * it + 2) * 4096;
        attn_block<false>(o0, o1, o2, o3, sa, q, ka0, ka1, va0, va1, kb0, kb1, vb0, vb1, vo, wvoff, wp, wl, ga, ga + 4096);
        if (it == 0) __builtin_amdgcn_s_setprio(2); else if (it == 1) __builtin_amdgcn_s_setprio(1); else __builtin_amdgcn_s_setprio(0);
        RESCALE_CHECK();
    }
    attn_block<true>(o0, o1, o2, o3, sa, q, ka0, ka1, va0, va1, kb0, kb1, vb0, vb1, vo, wvoff, wp, wl, kvw, kvw);
    STAMP(4);
    { const bool b0 = o_bad(o0, hh), b1 = o_bad(o1, hh), b2 = o_bad(o2, hh), b3 = o_bad(o3, hh);
      if (__any(b0 || b1 || b2 || b3)) {
          if (__any(b0)) attn_safe(kvw, lane, q[0], q[1], o0, mr0);
          if (__any(b1)) attn_safe(kvw, lane, q[2], q[3], o1, mr1);
          if (__any(b2)) attn_safe(kvw, lane, q[4], q[5], o2, mr2);
          if (__any(b3)) attn_safe(kvw, lane, q[6], q[7], o3, mr3);
      } }
    STAMP(11);
    { lptr mb = lds + L_MB + w * 1024 + vo;
      *(LAS bf16x8*)(mb + 0 * 8192) = pack8(o0, 0); *(LAS bf16x8*)(mb + 4 * 8192) = pack8(o0, 1);
      *(LAS bf16x8*)(mb + 1 * 8192) = pack8(o1, 0); *(LAS bf16x8*)(mb + 5 * 8192) = pack8(o1, 1);
      *(LAS bf16x8*)(mb + 2 * 8192) = pack8(o2, 0); *(LAS bf16x8*)(mb + 6 * 8192) = pack8(o2, 1);
      *(LAS bf16x8*)(mb + 3 * 8192) = pack8(o3, 0); *(LAS bf16x8*)(mb + 7 * 8192) = pack8(o3, 1);
      if (hh) { lptr ml = lds + L_ML + w * 1024 + r * 8;
          *(LAS f32x2*)(ml) = (f32x2){mr0, o0[12]}; *(LAS f32x2*)(ml + 256) = (f32x2){mr1, o1[12]}; *(LAS f32x2*)(ml + 512) = (f32x2){mr2, o2[12]}; *(LAS f32x2*)(ml + 768) = (f32x2){mr3, o3[12]}; } }
    bar();
    STAMP(5);
    { const int qi = w & 3, ks = w >> 2;
      f32x2 ml[8]; bf16x8 fr[8];
#pragma unroll
      for (int s = 0; s < 8; ++s) { ml[s] = *(LAS f32x2*)(lds + L_ML + s * 1024 + qi * 256 + r * 8); fr[s] = lds16(lds + L_MB + w * 8192 + s * 1024 + vo); }
      bool same = true;
#pragma unroll
      for (int s = 1; s < 8; ++s) same = same && ml[s][0] == ml[0][0];
      float z[8], l;
      if (__all(same)) {
          bf16x8 sel;
#pragma unroll
          for (int jj = 0; jj < 8; ++jj) sel[jj] = (r == 16 * ks + 8 * (jj >> 2) + 4 * hh + (jj & 3)) ? (short)0x3F80 : (short)0;
          f32x16 d0 = MFMA(sel, fr[0], zero), d1 = MFMA(sel, fr[1], zero);
          d0 = MFMA(sel, fr[2], d0); d1 = MFMA(sel, fr[3], d1); d0 = MFMA(sel, fr[4], d0); d1 = MFMA(sel, fr[5], d1); d0 = MFMA(sel, fr[6], d0); d1 = MFMA(sel, fr[7], d1);
          l = ((ml[0][1] + ml[1][1]) + (ml[2][1] + ml[3][1])) + ((ml[4][1] + ml[5][1]) + (ml[6][1] + ml[7][1]));
#pragma unroll
          for (int jj = 0; jj < 8; ++jj) z[jj] = ks ? d0[8 + jj] + d1[8 + jj] : d0[jj] + d1[jj];
      } else {
          float M = ml[0][0];
#pragma unroll
          for (int s = 1; s < 8; ++s) M = __builtin_fmaxf(M, ml[s][0]);
          l = 0.f;
#pragma unroll
          for (int jj = 0; jj < 8; ++jj) z[jj] = 0.f;
#pragma unroll
          for (int s = 0; s < 8; ++s) { const float f = __builtin_amdgcn_exp2f(ml[s][0] - M); l += f * ml[s][1]; const u32x4 u = __builtin_bit_cast(u32x4, fr[s]);
#pragma unroll
              for (int jj = 0; jj < 4; ++jj) { z[2 * jj] += f * __uint_as_float(u[jj] << 16); z[2 * jj + 1] += f * __uint_as_float(u[jj] & 0xffff0000u); } }
      }
      const float inv = 1.f / l;
      u32x4 u = {pk(z[0] * inv, z[1] * inv), pk(z[2] * inv, z[3] * inv), pk(z[4] * inv, z[5] * inv), pk(z[6] * inv, z[7] * inv)};
      if (ks && hh) { u[2] = 0; u[3] = 0; }
      st_handoff(rs, WS_O + (((qb * 4 + qi) * 16 + h) * 2 + ks) * 1024 + vo, __builtin_bit_cast(bf16x8, u)); }
    STAMP(6);
    bar();
    if (w < 7) { const char* wt = p.woa + ocg * 224 + 384 * XROW; const unsigned lt = lds0 + L_WA + 384 * 224 + w * 1024; glds(wt, wvoff, lt); glds(wt + 32 * XROW, wvoff, lt + 7168); }
    { const int cb = (56 * h) & ~15;
      for (int i = w; i < 28; i += 8) glds(p.wob + cb + (size_t)i * 16 * XROW, (lane >> 2) * XROW + (lane & 3) * 16, lds0 + L_WB + i * 1024); }
    if (tid < 112) *(LAS u32x4*)(lds + L_WA_END + tid * 16) = (u32x4){0, 0, 0, 0};
    if (tid >= 128 && tid < 144) *(LAS u32x4*)(lds + L_WB_END + (tid - 128) * 16) = (u32x4){0, 0, 0, 0};
    const int blk = (lane >> 4) & 1, q4 = (lane & 15) >> 2, pp = lane & 3;
    { const int rbl = w & 3, kh = w >> 2;
      u32x4 of[16];
      ld_handoff<16>(rs, WS_O + WS_OBR + ((qb * 4 + rbl) * 16 + kh * 8) * 2048 + vo, of);
      wait_vm<0>(); bar();
      STAMP(7);
      lptr wb = lds + L_WB + (kh * 8 * 28 + 4 * hh + q4) * 64 + ((56 * h) & 15) + 32 * blk + 8 * pp;
      f32x16 acc = {};
#pragma unroll
      for (int hi = 0; hi < 8; ++hi)
#pragma unroll
          for (int ks = 0; ks < 2; ++ks) acc = MFMA(ldtr8(wb + (hi * 28 + ks * 16) * 64, 8 * 64), __builtin_bit_cast(bf16x8, of[hi * 2 + ks]), acc);
      lptr ex = lds + L_EX + rbl * 4096 + vo;
      if (kh) {
#pragma unroll
          for (int i = 0; i < 4; ++i) *(LAS f32x4*)(ex + i * 1024) = (f32x4){acc[4 * i], acc[4 * i + 1], acc[4 * i + 2], acc[4 * i + 3]};
      }
      bar();
      if (!kh) {
          char* dst = p.out + (size_t)((qb * 4 + rbl) * 32 + r) * OROW + 896 + h * 56 + hh * 8;
#pragma unroll
          for (int i = 0; i < 4; ++i) {
              const f32x4 y = *(LAS f32x4*)(ex + i * 1024);
              if (8 * i + 4 * hh < 28) *(u32x2*)(dst + i * 16) = (u32x2){pk(acc[4 * i] + y[0], acc[4 * i + 1] + y[1]), pk(acc[4 * i + 2] + y[2], acc[4 * i + 3] + y[3])};
          }
      } }
    STAMP(8);
    { u32x4 v[4];
      ld_handoff<4>(rs, WS_O + orb * 32768 + w * 4096 + vo, v);
#pragma unroll
      for (int i = 0; i < 4; ++i) *(LAS u32x4*)(lds + L_OA + w * 4096 + i * 1024 + vo) = v[i];
      bar();
      STAMP(9);
      const int cbk = w & 3, kh = w >> 2, colb = 64 * cbk + 32 * blk + 8 * pp;
      lptr we = lds + L_WA + (hh + 2 * q4) * 224 + colb, wod = lds + L_WA + ((hh ? 4 : -3) + 2 * q4) * 224 + colb;
      lptr ob = lds + L_OA + vo + kh * 16384;
      f32x16 acc = {};
#pragma unroll
      for (int hi = 0; hi < 8; ++hi) {
          lptr wb = ((hi & 1) ? wod : we) + (kh * 8 + hi) * 28 * 224;
#pragma unroll
          for (int ks = 0; ks < 2; ++ks) acc = MFMA(ldtr8(wb + ks * 16 * 224, 8 * 224), lds16(ob + (hi * 2 + ks) * 1024), acc);
      }
      lptr ex = lds + L_EX + cbk * 4096 + vo;
      if (kh) {
#pragma unroll
          for (int i = 0; i < 4; ++i) *(LAS f32x4*)(ex + i * 1024) = (f32x4){acc[4 * i], acc[4 * i + 1], acc[4 * i + 2], acc[4 * i + 3]};
      }
      bar();
      if (!kh) {
          char* dst = p.out + (size_t)(orb * 32 + r) * OROW + ocg * 224 + cbk * 64 + hh * 8;
#pragma unroll
          for (int i = 0; i < 4; ++i) {
              const f32x4 y = *(LAS f32x4*)(ex + i * 1024);
              if (cbk * 32 + 8 * i + 4 * hh < 112) *(u32x2*)(dst + i * 16) = (u32x2){pk(acc[4 * i] + y[0], acc[4 * i + 1] + y[1]), pk(acc[4 * i + 2] + y[2], acc[4 * i + 3] + y[3])};
          }
      } }
    STAMP(10); RSTAMP(23); ST_OUT(1);
}

extern "C" void kernel_launch(void* const* d_in, const int* in_sizes, int n_in, void* d_out, int out_size, void* d_ws, size_t ws_size, hipStream_t stream) {
    Params p;
    p.xa = (const char*)d_in[0]; p.wa = (const char*)d_in[1]; p.woa = (const char*)d_in[2];
    p.xb = (const char*)d_in[3]; p.wb = (const char*)d_in[4]; p.wob = (const char*)d_in[5];
    p.out = (char*)d_out; p.ws = (char*)d_ws;
    (void)hipFuncSetAttribute((const void*)k_qkv, hipFuncAttributeMaxDynamicSharedMemorySize, LDS_BYTES);
    (void)hipFuncSetAttribute((const void*)k_attn, hipFuncAttributeMaxDynamicSharedMemorySize, LDS_BYTES);
    k_qkv<<<256, 1024, LDS_BYTES, stream>>>(p);
    k_attn<<<256, 512, LDS_BYTES, stream>>>(p);
}
```

```cpp
#include <hip/hip_runtime.h>
#include <stdint.h>

typedef short bf16x8 __attribute__((ext_vector_type(8)));
typedef short s16x4 __attribute__((ext_vector_type(4)));
typedef float f32x16 __attribute__((ext_vector_type(16)));
typedef float f32x4 __attribute__((ext_vector_type(4)));
typedef float f32x2 __attribute__((ext_vector_type(2)));
typedef unsigned u32x4 __attribute__((ext_vector_type(4)));
typedef unsigned u32x2 __attribute__((ext_vector_type(2)));
typedef __bf16 bf2_t __attribute__((ext_vector_type(2)));
typedef unsigned long long u64;
#define LAS __attribute__((address_space(3)))
typedef LAS char* lptr;
typedef __attribute__((address_space(1))) u64 gu64;
typedef __amdgpu_buffer_rsrc_t rsrc_t;
#define DI __device__ __forceinline__
#define MFMA(a, b, c) __builtin_amdgcn_mfma_f32_32x32x16_bf16((a), (b), (c), 0, 0, 0)
#define SBAR() __builtin_amdgcn_sched_barrier(0)
#define PIN(x) asm volatile("" : "+v"(x))

constexpr int XROW = 896, WROW = 2688, OROW = 1792;
constexpr float QS = 0.18898223650461362f * 1.4426950408889634f;
constexpr unsigned WS_Q = 0, WS_KV = 2u << 20, WS_BR = 6u << 20, WS_O = 12u << 20, WS_OBR = 2u << 20, WS_FLG = 16u << 20, WS_END = 17u << 20;
constexpr unsigned F_A = 0, F_B = 2048, F_OA = 4096, F_OB = 6144, F_GEN = 8192;
constexpr int LDS_BYTES = 160 * 1024;
#define OUT_AUX 16

DI unsigned pk(float a, float b) { f32x2 v = {a, b}; return __builtin_bit_cast(unsigned, __builtin_convertvector(v, bf2_t)); }
DI bf16x8 pack8(const f32x16& x, int s) {
    u32x4 p = {pk(x[8 * s], x[8 * s + 1]), pk(x[8 * s + 2], x[8 * s + 3]), pk(x[8 * s + 4], x[8 * s + 5]), pk(x[8 * s + 6], x[8 * s + 7])};
    return __builtin_bit_cast(bf16x8, p);
}
DI void glds(const void* sbase, unsigned voff, unsigned ldst) {
    unsigned sv;
    asm volatile("s_mov_b32 %0, m0\n\ts_mov_b32 m0, %3\n\ts_nop 2\n\tglobal_load_lds_dwordx4 %1, %2\n\ts_mov_b32 m0, %0"
                 : "=&s"(sv) : "v"(voff), "s"(sbase), "s"(ldst) : "memory");
}
DI void touch(const void* a, unsigned ldst) {
    unsigned sv;
    asm volatile("s_mov_b32 %0, m0\n\ts_mov_b32 m0, %2\n\ts_nop 0\n\tglobal_load_lds_dword %1, off\n\ts_mov_b32 m0, %0" : "=&s"(sv) : "v"(a), "s"(ldst) : "memory");
}
template <int N> DI void wait_vm() { asm volatile("s_waitcnt vmcnt(%0)" ::"n"(N) : "memory"); }
DI void bar() { asm volatile("s_waitcnt lgkmcnt(0)\n\ts_barrier" ::: "memory"); }
DI bf16x8 lds16(lptr p) { return *(const LAS bf16x8*)p; }
typedef short v4i16_t __attribute__((ext_vector_type(4)));
DI s16x4 ldtr(lptr p) { return __builtin_bit_cast(s16x4, __builtin_amdgcn_ds_read_tr16_b64_v4i16((LAS v4i16_t*)p)); }
DI bf16x8 ldtr8(lptr p, int stride4) { s16x4 lo = ldtr(p), hi = ldtr(p + stride4); return __builtin_shufflevector(lo, hi, 0, 1, 2, 3, 4, 5, 6, 7); }
DI float swap_hi(float x) { auto r = __builtin_amdgcn_permlane32_swap(__float_as_uint(x), __float_as_uint(x), false, false); return __uint_as_float(r[1]); }
DI float halfmax(float x) { auto r = __builtin_amdgcn_permlane32_swap(__float_as_uint(x), __float_as_uint(x), false, false); return __builtin_fmaxf(__uint_as_float(r[0]), __uint_as_float(r[1])); }
DI float max16(const f32x16& p) {
    float a = __builtin_fmaxf(__builtin_fmaxf(p[0], p[1]), p[2]), b = __builtin_fmaxf(__builtin_fmaxf(p[3], p[4]), p[5]);
    a = __builtin_fmaxf(__builtin_fmaxf(a, p[6]), p[7]); b = __builtin_fmaxf(__builtin_fmaxf(b, p[8]), p[9]);
    a = __builtin_fmaxf(__builtin_fmaxf(a, p[10]), p[11]); b = __builtin_fmaxf(__builtin_fmaxf(b, p[12]), p[13]);
    a = __builtin_fmaxf(__builtin_fmaxf(a, p[14]), p[15]);
    return __builtin_fmaxf(a, b);
}
DI rsrc_t ws_rsrc(char* ws) { return __builtin_amdgcn_make_buffer_rsrc(ws, 0, WS_END, 0x00020000); }
DI void st_dev(rsrc_t rs, unsigned off, bf16x8 v) { __builtin_amdgcn_raw_buffer_store_b128(__builtin_bit_cast(u32x4, v), rs, off, 0, 16); }
DI bf16x8 ld_dev(rsrc_t rs, unsigned off) { return __builtin_bit_cast(bf16x8, __builtin_amdgcn_raw_buffer_load_b128(rs, off, 0, 16)); }
DI gu64* flag_ptr(char* ws, unsigned off) { return (gu64*)(ws + WS_FLG + off); }
DI void flag_set(char* ws, unsigned off, u64 tag) { __hip_atomic_store(flag_ptr(ws, off), tag, __ATOMIC_RELAXED, __HIP_MEMORY_SCOPE_AGENT); }
DI u64 flag_get(char* ws, unsigned off) { return __hip_atomic_load(flag_ptr(ws, off), __ATOMIC_RELAXED, __HIP_MEMORY_SCOPE_AGENT); }
DI void flags_wait(char* ws, unsigned off, u64 tag) {
#pragma unroll 1
    for (int spin = 0; spin < (1 << 16); ++spin) { if (__all(flag_get(ws, off) == tag)) break; __builtin_amdgcn_s_sleep(1); }
}

struct Params { const char *xa, *wa, *woa, *xb, *wb, *wob; char* out; char* ws; };

constexpr int G_XB = 16 * 1024, G_STAGE = 28 * 1024, G_NST = 4;
DI void g_pack(f32x16 a, int slot, int lane, float qs, bf16x8& f0, bf16x8& f1) {
    const int hh = lane >> 5;
    if (slot < 16) {
#pragma unroll
        for (int r = 0; r < 16; ++r) a[r] *= qs;
        if (hh) { a[12] = 0.f; a[13] = 0.f; a[14] = 0.f; a[15] = 0.f; }
    } else if (slot < 32) {
        if (hh) { a[12] = 1.f; a[13] = 0.f; a[14] = 0.f; a[15] = 0.f; }
    } else {
        const int d = lane & 31;
        if (d >= 28) {
            const float c = d == 28 ? 1.f : 0.f;
#pragma unroll
            for (int r = 0; r < 16; ++r) a[r] = c;
        }
    }
    f0 = pack8(a, 0); f1 = pack8(a, 1);
}
DI void g_put(rsrc_t rs, unsigned wsb, int slot, int rb, int lane, bf16x8 f0, bf16x8 f1) {
    const unsigned dst = wsb + (slot < 16 ? WS_Q + ((slot * 64 + rb) * 2) * 1024 : slot < 32 ? WS_KV + (((slot - 16) * 64 + rb) * 4) * 1024 : WS_KV + (((slot - 32) * 64 + rb) * 4 + 2) * 1024) + lane * 16;
    st_dev(rs, dst, f0); st_dev(rs, dst + 1024, f1);
}
template <bool V2, bool V01>
DI void g_chunk(lptr xs, int cx, lptr ws_, f32x16& a0, f32x16& a1, f32x16& a2) {
    bf16x8 xf[4], w0[4], w1[4], w2[4];
#pragma unroll
    for (int ks = 0; ks < 4; ++ks) {
        xf[ks] = lds16(xs + ((cx ^ (2 * ks)) << 4));
        w0[ks] = ldtr8(ws_ + ks * 3072, 768); w1[ks] = ldtr8(ws_ + ks * 3072 + 56, 768); w2[ks] = ldtr8(ws_ + ks * 3072 + 112, 768);
    }
#pragma unroll
    for (int ks = 0; ks < 4; ++ks) {
        a0 = V01 ? MFMA(xf[ks], w0[ks], a0) : MFMA(w0[ks], xf[ks], a0);
        a1 = V01 ? MFMA(xf[ks], w1[ks], a1) : MFMA(w1[ks], xf[ks], a1);
        a2 = V2 ? MFMA(xf[ks], w2[ks], a2) : MFMA(w2[ks], xf[ks], a2);
    }
}

DI float sel_ninf(unsigned m, int bit, float x) {
    const unsigned k = (unsigned)__builtin_amdgcn_sbfe((int)m, bit, 1);
    return __uint_as_float((__float_as_uint(x) & k) | (0xFF800000u & ~k));
}
DI void local_unit(rsrc_t rs, int rb, int h, int lane) {
    const int hh = lane >> 5, r = lane & 31, rp = rb > 0 ? rb - 1 : 0;
    const unsigned qs = WS_BR + WS_Q + ((h * 64 + rb) * 2) * 1024 + lane * 16, kc = WS_BR + WS_KV + ((h * 64 + rb) * 4) * 1024 + lane * 16, kp = WS_BR + WS_KV + ((h * 64 + rp) * 4) * 1024 + lane * 16;
    const bf16x8 q0 = ld_dev(rs, qs), q1 = ld_dev(rs, qs + 1024), kp0 = ld_dev(rs, kp), kp1 = ld_dev(rs, kp + 1024), kc0 = ld_dev(rs, kc), kc1 = ld_dev(rs, kc + 1024);
    const bf16x8 vp1 = ld_dev(rs, kp + 3072), vc0 = ld_dev(rs, kc + 2048), vc1 = ld_dev(rs, kc + 3072);
    const unsigned mc = (unsigned)((0x5555ull << r) >> 14) >> (4 * hh), mp = rb == 0 ? 0u : (unsigned)(0x5555ull << (r + 18)) >> (4 * hh);
    f32x16 sp = {}, sc = {};
    sp = MFMA(kp0, q0, sp); sc = MFMA(kc0, q0, sc); sp = MFMA(kp1, q1, sp); sc = MFMA(kc1, q1, sc);
#pragma unroll
    for (int i = 0; i < 16; ++i) { const int c = (i & 3) + 8 * (i >> 2); sc[i] = sel_ninf(mc, c, sc[i]); sp[i] = i < 8 ? -INFINITY : sel_ninf(mp, c, sp[i]); }
    float mx = max16(sc);
#pragma unroll
    for (int i = 8; i < 16; i += 2) mx = __builtin_fmaxf(__builtin_fmaxf(mx, sp[i]), sp[i + 1]);
    const float m = -QS * halfmax(mx);
#pragma unroll
    for (int i = 0; i < 16; ++i) { sc[i] = __builtin_amdgcn_exp2f(__builtin_fmaf(sc[i], QS, m)); if (i >= 8) sp[i] = __builtin_amdgcn_exp2f(__builtin_fmaf(sp[i], QS, m)); }
    f32x16 o = {}, o2 = {};
    o = MFMA(vp1, pack8(sp, 1), o); o2 = MFMA(vc0, pack8(sc, 0), o2); o = MFMA(vc1, pack8(sc, 1), o);
    const float inv = __builtin_amdgcn_rcpf(swap_hi(o[12] + o2[12]));
#pragma unroll
    for (int i = 0; i < 16; ++i) o[i] = (o[i] + o2[i]) * inv;
    if (hh) { o[12] = o[13] = o[14] = o[15] = 0.f; }
    const unsigned dst = WS_O + WS_OBR + ((rb * 16 + h) * 2) * 1024 + lane * 16;
    st_dev(rs, dst, pack8(o, 0)); st_dev(rs, dst + 1024, pack8(o, 1));
}

constexpr int L_WB = 0;
constexpr int L_WA = 448 * 128, L_WAH = L_WA + 224 * 128;
constexpr int L_MB = L_WAH, L_ML = L_MB + 65536;
constexpr int L_Q = G_NST * G_STAGE, L_K0 = L_Q + 8192, L_M0 = L_K0 + 2048;
constexpr int L_CNT = L_ML + 8192;
constexpr int L_TR = 40960;
constexpr int L_TOUCH = L_CNT + 1024;
constexpr int L_OB = L_MB;
constexpr int L_OA0 = 0, L_OA1 = L_WA + 448 * 128, L_EX = 147456;

#define ATT_IO \
          "+{v[0:15]}"(o0), "+{v[16:31]}"(o1), "+{v[32:47]}"(o2), "+{v[48:63]}"(o3), "+{v[64:79]}"(sa), \
          "+{v[128:131]}"(kv[0]), "+{v[132:135]}"(kv[1]), "+{v[136:139]}"(kv[2]), "+{v[140:143]}"(kv[3]), \
          "+{v[144:147]}"(kv[4]), "+{v[148:151]}"(kv[5]), "+{v[152:155]}"(kv[6]), "+{v[156:159]}"(kv[7]), \
          "+{v[180:183]}"(kv[8]), "+{v[184:187]}"(kv[9]), "+{v[188:191]}"(kv[10]), "+{v[192:195]}"(kv[11]), \
          "+{v[196:199]}"(kv[12]), "+{v[200:203]}"(kv[13]), "+{v[204:207]}"(kv[14]), "+{v[208:211]}"(kv[15]), \
          "+{s[40:41]}"(wp), "+{s42}"(wl), "={s43}"(m0save)
#define ATT_IN \
          "{v[96:99]}"(q[0]), "{v[100:103]}"(q[1]), "{v[104:107]}"(q[2]), "{v[108:111]}"(q[3]), \
          "{v[112:115]}"(q[4]), "{v[116:119]}"(q[5]), "{v[120:123]}"(q[6]), "{v[124:127]}"(q[7]), \
          "{v176}"(voff), "{v177}"(wvoff), "{s[44:45]}"(ga), "{s[46:47]}"(gb), "{s48}"(ginc), "{s49}"(linc)
#define ATT_CLOB "memory", "scc", \
          "v80", "v81", "v82", "v83", "v84", "v85", "v86", "v87", "v88", "v89", "v90", "v91", "v92", "v93", "v94", "v95", \
          "v160", "v161", "v162", "v163", "v164", "v165", "v166", "v167", "v168", "v169", "v170", "v171", "v172", "v173", "v174", "v175"
template <int IDX> DI void attn_block(f32x16& o0, f32x16& o1, f32x16& o2, f32x16& o3, f32x16& sa, const bf16x8 (&q)[8], bf16x8 (&kv)[16],
                   unsigned voff, unsigned wvoff, const char*& wp, unsigned& wl, unsigned ginc, unsigned linc, const char* ga, const char* gb) {
    unsigned m0save;
    if (IDX == 0) {
        asm volatile(
    "s_mov_b32 s43, m0\n\t"
    "v_mfma_f32_32x32x16_bf16 v[80:95], v[128:131], v[104:107], 0\n\t"
    "v_exp_f32 v64, v64\n\t"
    "v_exp_f32 v65, v65\n\t"
    "v_exp_f32 v66, v66\n\t"
    "v_exp_f32 v67, v67\n\t"
    "v_cvt_pk_bf16_f32 v160, v64, v65\n\t"
    "v_cvt_pk_bf16_f32 v161, v66, v67\n\t"
    "global_load_dwordx4 v[180:183], v176, s[44:45] offset:0 sc1\n\t"
    "v_mfma_f32_32x32x16_bf16 v[80:95], v[132:135], v[108:111], v[80:95]\n\t"
    "v_exp_f32 v68, v68\n\t"
    "v_exp_f32 v69, v69\n\t"
    "v_exp_f32 v70, v70\n\t"
    "v_exp_f32 v71, v71\n\t"
    "v_cvt_pk_bf16_f32 v162, v68, v69\n\t"
    "v_cvt_pk_bf16_f32 v163, v70, v71\n\t"
    "global_load_dwordx4 v[184:187], v176, s[44:45] offset:1024 sc1\n\t"
    "v_exp_f32 v72, v72\n\t"
    "v_exp_f32 v73, v73\n\t"
    "v_exp_f32 v74, v74\n\t"
    "v_exp_f32 v75, v75\n\t"
    "v_cvt_pk_bf16_f32 v164, v72, v73\n\t"
    "v_cvt_pk_bf16_f32 v165, v74, v75\n\t"
    "v_exp_f32 v76, v76\n\t"
    "v_exp_f32 v77, v77\n\t"
    "v_exp_f32 v78, v78\n\t"
    "v_exp_f32 v79, v79\n\t"
    "v_cvt_pk_bf16_f32 v166, v76, v77\n\t"
    "v_cvt_pk_bf16_f32 v167, v78, v79\n\t"
    "v_mfma_f32_32x32x16_bf16 v[64:79], v[128:131], v[112:115], 0\n\t"
    "v_exp_f32 v80, v80\n\t"
    "v_exp_f32 v81, v81\n\t"
    "v_exp_f32 v82, v82\n\t"
    "v_exp_f32 v83, v83\n\t"
    "v_cvt_pk_bf16_f32 v168, v80, v81\n\t"
    "v_cvt_pk_bf16_f32 v169, v82, v83\n\t"
    "global_load_dwordx4 v[188:191], v176, s[44:45] offset:2048 sc1\n\t"
    "v_mfma_f32_32x32x16_bf16 v[64:79], v[132:135], v[116:119], v[64:79]\n\t"
    "v_exp_f32 v84, v84\n\t"
    "v_exp_f32 v85, v85\n\t"
    "v_exp_f32 v86, v86\n\t"
    "v_exp_f32 v87, v87\n\t"
    "v_cvt_pk_bf16_f32 v170, v84, v85\n\t"
    "v_cvt_pk_bf16_f32 v171, v86, v87\n\t"
    "global_load_dwordx4 v[192:195], v176, s[44:45] offset:3072 sc1\n\t"
    "v_mfma_f32_32x32x16_bf16 v[0:15], v[136:139], v[160:163], v[0:15]\n\t"
    "v_exp_f32 v88, v88\n\t"
    "v_exp_f32 v89, v89\n\t"
    "v_exp_f32 v90, v90\n\t"
    "v_exp_f32 v91, v91\n\t"
    "v_cvt_pk_bf16_f32 v172, v88, v89\n\t"
    "v_cvt_pk_bf16_f32 v173, v90, v91\n\t"
    "v_mfma_f32_32x32x16_bf16 v[0:15], v[140:143], v[164:167], v[0:15]\n\t"
    "v_exp_f32 v92, v92\n\t"
    "v_exp_f32 v93, v93\n\t"
    "v_exp_f32 v94, v94\n\t"
    "v_exp_f32 v95, v95\n\t"
    "v_cvt_pk_bf16_f32 v174, v92, v93\n\t"
    "v_cvt_pk_bf16_f32 v175, v94, v95\n\t"
    "v_mfma_f32_32x32x16_bf16 v[80:95], v[128:131], v[120:123], 0\n\t"
    "v_exp_f32 v64, v64\n\t"
    "v_exp_f32 v65, v65\n\t"
    "v_exp_f32 v66, v66\n\t"
    "v_exp_f32 v67, v67\n\t"
    "v_cvt_pk_bf16_f32 v160, v64, v65\n\t"
    "v_cvt_pk_bf16_f32 v161, v66, v67\n\t"
    "global_load_dwordx4 v[196:199], v176, s[46:47] offset:0 sc1\n\t"
    "v_mfma_f32_32x32x16_bf16 v[80:95], v[132:135], v[124:127], v[80:95]\n\t"
    "v_exp_f32 v68, v68\n\t"
    "v_exp_f32 v69, v69\n\t"
    "v_exp_f32 v70, v70\n\t"
    "v_exp_f32 v71, v71\n\t"
    "v_cvt_pk_bf16_f32 v162, v68, v69\n\t"
    "v_cvt_pk_bf16_f32 v163, v70, v71\n\t"
    "global_load_dwordx4 v[200:203], v176, s[46:47] offset:1024 sc1\n\t"
    "v_mfma_f32_32x32x16_bf16 v[16:31], v[136:139], v[168:171], v[16:31]\n\t"
    "v_exp_f32 v72, v72\n\t"
    "v_exp_f32 v73, v73\n\t"
    "v_exp_f32 v74, v74\n\t"
    "v_exp_f32 v75, v75\n\t"
    "v_cvt_pk_bf16_f32 v164, v72, v73\n\t"
    "v_cvt_pk_bf16_f32 v165, v74, v75\n\t"
    "v_mfma_f32_32x32x16_bf16 v[16:31], v[140:143], v[172:175], v[16:31]\n\t"
    "v_exp_f32 v76, v76\n\t"
    "v_exp_f32 v77, v77\n\t"
    "v_exp_f32 v78, v78\n\t"
    "v_exp_f32 v79, v79\n\t"
    "v_cvt_pk_bf16_f32 v166, v76, v77\n\t"
    "v_cvt_pk_bf16_f32 v167, v78, v79\n\t"
    "v_mfma_f32_32x32x16_bf16 v[64:79], v[144:147], v[96:99], 0\n\t"
    "v_exp_f32 v80, v80\n\t"
    "v_exp_f32 v81, v81\n\t"
    "v_exp_f32 v82, v82\n\t"
    "v_exp_f32 v83, v83\n\t"
    "v_cvt_pk_bf16_f32 v168, v80, v81\n\t"
    "v_cvt_pk_bf16_f32 v169, v82, v83\n\t"
    "global_load_dwordx4 v[204:207], v176, s[46:47] offset:2048 sc1\n\t"
    "v_mfma_f32_32x32x16_bf16 v[64:79], v[148:151], v[100:103], v[64:79]\n\t"
    "v_exp_f32 v84, v84\n\t"
    "v_exp_f32 v85, v85\n\t"
    "v_exp_f32 v86, v86\n\t"
    "v_exp_f32 v87, v87\n\t"
    "v_cvt_pk_bf16_f32 v170, v84, v85\n\t"
    "v_cvt_pk_bf16_f32 v171, v86, v87\n\t"
    "global_load_dwordx4 v[208:211], v176, s[46:47] offset:3072 sc1\n\t"
    "v_mfma_f32_32x32x16_bf16 v[32:47], v[136:139], v[160:163], v[32:47]\n\t"
    "v_exp_f32 v88, v88\n\t"
    "v_exp_f32 v89, v89\n\t"
    "v_exp_f32 v90, v90\n\t"
    "v_exp_f32 v91, v91\n\t"
    "v_cvt_pk_bf16_f32 v172, v88, v89\n\t"
    "v_cvt_pk_bf16_f32 v173, v90, v91\n\t"
    "v_mfma_f32_32x32x16_bf16 v[32:47], v[140:143], v[164:167], v[32:47]\n\t"
    "v_exp_f32 v92, v92\n\t"
    "v_exp_f32 v93, v93\n\t"
    "v_exp_f32 v94, v94\n\t"
    "v_exp_f32 v95, v95\n\t"
    "v_cvt_pk_bf16_f32 v174, v92, v93\n\t"
    "v_cvt_pk_bf16_f32 v175, v94, v95\n\t"
    "v_mfma_f32_32x32x16_bf16 v[80:95], v[144:147], v[104:107], 0\n\t"
    "v_exp_f32 v64, v64\n\t"
    "v_exp_f32 v65, v65\n\t"
    "v_exp_f32 v66, v66\n\t"
    "v_exp_f32 v67, v67\n\t"
    "v_cvt_pk_bf16_f32 v160, v64, v65\n\t"
    "v_cvt_pk_bf16_f32 v161, v66, v67\n\t"
    "s_mov_b32 m0, s42\n\t"
    "s_add_u32 s42, s42, s49\n\t"
    "global_load_lds_dwordx4 v177, s[40:41]\n\t"
    "s_add_u32 s40, s40, s48\n\t"
    "s_addc_u32 s41, s41, 0\n\t"
    "v_mfma_f32_32x32x16_bf16 v[80:95], v[148:151], v[108:111], v[80:95]\n\t"
    "v_exp_f32 v68, v68\n\t"
    "v_exp_f32 v69, v69\n\t"
    "v_exp_f32 v70, v70\n\t"
    "v_exp_f32 v71, v71\n\t"
    "v_cvt_pk_bf16_f32 v162, v68, v69\n\t"
    "v_cvt_pk_bf16_f32 v163, v70, v71\n\t"
    "v_mfma_f32_32x32x16_bf16 v[48:63], v[136:139], v[168:171], v[48:63]\n\t"
    "v_exp_f32 v72, v72\n\t"
    "v_exp_f32 v73, v73\n\t"
    "v_exp_f32 v74, v74\n\t"
    "v_exp_f32 v75, v75\n\t"
    "v_cvt_pk_bf16_f32 v164, v72, v73\n\t"
    "v_cvt_pk_bf16_f32 v165, v74, v75\n\t"
    "v_mfma_f32_32x32x16_bf16 v[48:63], v[140:143], v[172:175], v[48:63]\n\t"
    "v_exp_f32 v76, v76\n\t"
    "v_exp_f32 v77, v77\n\t"
    "v_exp_f32 v78, v78\n\t"
    "v_exp_f32 v79, v79\n\t"
    "v_cvt_pk_bf16_f32 v166, v76, v77\n\t"
    "v_cvt_pk_bf16_f32 v167, v78, v79\n\t"
    "v_mfma_f32_32x32x16_bf16 v[64:79], v[144:147], v[112:115], 0\n\t"
    "v_exp_f32 v80, v80\n\t"
    "v_exp_f32 v81, v81\n\t"
    "v_exp_f32 v82, v82\n\t"
    "v_exp_f32 v83, v83\n\t"
    "v_cvt_pk_bf16_f32 v168, v80, v81\n\t"
    "v_cvt_pk_bf16_f32 v169, v82, v83\n\t"
    "s_mov_b32 m0, s42\n\t"
    "s_add_u32 s42, s42, s49\n\t"
    "global_load_lds_dwordx4 v177, s[40:41]\n\t"
    "s_add_u32 s40, s40, s48\n\t"
    "s_addc_u32 s41, s41, 0\n\t"
    "v_mfma_f32_32x32x16_bf16 v[64:79], v[148:151], v[116:119], v[64:79]\n\t"
    "v_exp_f32 v84, v84\n\t"
    "v_exp_f32 v85, v85\n\t"
    "v_exp_f32 v86, v86\n\t"
    "v_exp_f32 v87, v87\n\t"
    "v_cvt_pk_bf16_f32 v170, v84, v85\n\t"
    "v_cvt_pk_bf16_f32 v171, v86, v87\n\t"
    "v_mfma_f32_32x32x16_bf16 v[0:15], v[152:155], v[160:163], v[0:15]\n\t"
    "v_exp_f32 v88, v88\n\t"
    "v_exp_f32 v89, v89\n\t"
    "v_exp_f32 v90, v90\n\t"
    "v_exp_f32 v91, v91\n\t"
    "v_cvt_pk_bf16_f32 v172, v88, v89\n\t"
    "v_cvt_pk_bf16_f32 v173, v90, v91\n\t"
    "v_mfma_f32_32x32x16_bf16 v[0:15], v[156:159], v[164:167], v[0:15]\n\t"
    "v_exp_f32 v92, v92\n\t"
    "v_exp_f32 v93, v93\n\t"
    "v_exp_f32 v94, v94\n\t"
    "v_exp_f32 v95, v95\n\t"
    "v_cvt_pk_bf16_f32 v174, v92, v93\n\t"
    "v_cvt_pk_bf16_f32 v175, v94, v95\n\t"
    "v_mfma_f32_32x32x16_bf16 v[80:95], v[144:147], v[120:123], 0\n\t"
    "v_exp_f32 v64, v64\n\t"
    "v_exp_f32 v65, v65\n\t"
    "v_exp_f32 v66, v66\n\t"
    "v_exp_f32 v67, v67\n\t"
    "v_cvt_pk_bf16_f32 v160, v64, v65\n\t"
    "v_cvt_pk_bf16_f32 v161, v66, v67\n\t"
    "s_mov_b32 m0, s42\n\t"
    "s_add_u32 s42, s42, s49\n\t"
    "global_load_lds_dwordx4 v177, s[40:41]\n\t"
    "s_add_u32 s40, s40, s48\n\t"
    "s_addc_u32 s41, s41, 0\n\t"
    "v_mfma_f32_32x32x16_bf16 v[80:95], v[148:151], v[124:127], v[80:95]\n\t"
    "v_exp_f32 v68, v68\n\t"
    "v_exp_f32 v69, v69\n\t"
    "v_exp_f32 v70, v70\n\t"
    "v_exp_f32 v71, v71\n\t"
    "v_cvt_pk_bf16_f32 v162, v68, v69\n\t"
    "v_cvt_pk_bf16_f32 v163, v70, v71\n\t"
    "v_mfma_f32_32x32x16_bf16 v[16:31], v[152:155], v[168:171], v[16:31]\n\t"
    "v_exp_f32 v72, v72\n\t"
    "v_exp_f32 v73, v73\n\t"
    "v_exp_f32 v74, v74\n\t"
    "v_exp_f32 v75, v75\n\t"
    "v_cvt_pk_bf16_f32 v164, v72, v73\n\t"
    "v_cvt_pk_bf16_f32 v165, v74, v75\n\t"
    "v_mfma_f32_32x32x16_bf16 v[16:31], v[156:159], v[172:175], v[16:31]\n\t"
    "v_exp_f32 v76, v76\n\t"
    "v_exp_f32 v77, v77\n\t"
    "v_exp_f32 v78, v78\n\t"
    "v_exp_f32 v79, v79\n\t"
    "v_cvt_pk_bf16_f32 v166, v76, v77\n\t"
    "v_cvt_pk_bf16_f32 v167, v78, v79\n\t"
    "s_waitcnt vmcnt(9)\n\t"
    "v_mfma_f32_32x32x16_bf16 v[64:79], v[180:183], v[96:99], 0\n\t"
    "v_exp_f32 v80, v80\n\t"
    "v_exp_f32 v81, v81\n\t"
    "v_exp_f32 v82, v82\n\t"
    "v_exp_f32 v83, v83\n\t"
    "v_cvt_pk_bf16_f32 v168, v80, v81\n\t"
    "v_cvt_pk_bf16_f32 v169, v82, v83\n\t"
    "s_mov_b32 m0, s42\n\t"
    "s_add_u32 s42, s42, s49\n\t"
    "global_load_lds_dwordx4 v177, s[40:41]\n\t"
    "s_add_u32 s40, s40, s48\n\t"
    "s_addc_u32 s41, s41, 0\n\t"
    "v_mfma_f32_32x32x16_bf16 v[64:79], v[184:187], v[100:103], v[64:79]\n\t"
    "v_exp_f32 v84, v84\n\t"
    "v_exp_f32 v85, v85\n\t"
    "v_exp_f32 v86, v86\n\t"
    "v_exp_f32 v87, v87\n\t"
    "v_cvt_pk_bf16_f32 v170, v84, v85\n\t"
    "v_cvt_pk_bf16_f32 v171, v86, v87\n\t"
    "v_mfma_f32_32x32x16_bf16 v[32:47], v[152:155], v[160:163], v[32:47]\n\t"
    "v_exp_f32 v88, v88\n\t"
    "v_exp_f32 v89, v89\n\t"
    "v_exp_f32 v90, v90\n\t"
    "v_exp_f32 v91, v91\n\t"
    "v_cvt_pk_bf16_f32 v172, v88, v89\n\t"
    "v_cvt_pk_bf16_f32 v173, v90, v91\n\t"
    "v_mfma_f32_32x32x16_bf16 v[32:47], v[156:159], v[164:167], v[32:47]\n\t"
    "v_exp_f32 v92, v92\n\t"
    "v_exp_f32 v93, v93\n\t"
    "v_exp_f32 v94, v94\n\t"
    "v_exp_f32 v95, v95\n\t"
    "v_cvt_pk_bf16_f32 v174, v92, v93\n\t"
    "v_cvt_pk_bf16_f32 v175, v94, v95\n\t"
    "v_mfma_f32_32x32x16_bf16 v[48:63], v[152:155], v[168:171], v[48:63]\n\t"
    "s_nop 1\n\t"
    "v_mfma_f32_32x32x16_bf16 v[48:63], v[156:159], v[172:175], v[48:63]\n\t"
    "s_mov_b32 m0, s43\n\t"
    "s_nop 7\n\t"
    "s_nop 3"
        : ATT_IO : ATT_IN : ATT_CLOB);
    } else if (IDX == 1) {
        asm volatile(
    "s_mov_b32 s43, m0\n\t"
    "v_mfma_f32_32x32x16_bf16 v[80:95], v[180:183], v[104:107], 0\n\t"
    "v_exp_f32 v64, v64\n\t"
    "v_exp_f32 v65, v65\n\t"
    "v_exp_f32 v66, v66\n\t"
    "v_exp_f32 v67, v67\n\t"
    "v_cvt_pk_bf16_f32 v160, v64, v65\n\t"
    "v_cvt_pk_bf16_f32 v161, v66, v67\n\t"
    "global_load_dwordx4 v[128:131], v176, s[44:45] offset:0 sc1\n\t"
    "v_mfma_f32_32x32x16_bf16 v[80:95], v[184:187], v[108:111], v[80:95]\n\t"
    "v_exp_f32 v68, v68\n\t"
    "v_exp_f32 v69, v69\n\t"
    "v_exp_f32 v70, v70\n\t"
    "v_exp_f32 v71, v71\n\t"
    "v_cvt_pk_bf16_f32 v162, v68, v69\n\t"
    "v_cvt_pk_bf16_f32 v163, v70, v71\n\t"
    "global_load_dwordx4 v[132:135], v176, s[44:45] offset:1024 sc1\n\t"
    "v_exp_f32 v72, v72\n\t"
    "v_exp_f32 v73, v73\n\t"
    "v_exp_f32 v74, v74\n\t"
    "v_exp_f32 v75, v75\n\t"
    "v_cvt_pk_bf16_f32 v164, v72, v73\n\t"
    "v_cvt_pk_bf16_f32 v165, v74, v75\n\t"
    "v_exp_f32 v76, v76\n\t"
    "v_exp_f32 v77, v77\n\t"
    "v_exp_f32 v78, v78\n\t"
    "v_exp_f32 v79, v79\n\t"
    "v_cvt_pk_bf16_f32 v166, v76, v77\n\t"
    "v_cvt_pk_bf16_f32 v167, v78, v79\n\t"
    "v_mfma_f32_32x32x16_bf16 v[64:79], v[180:183], v[112:115], 0\n\t"
    "v_exp_f32 v80, v80\n\t"
    "v_exp_f32 v81, v81\n\t"
    "v_exp_f32 v82, v82\n\t"
    "v_exp_f32 v83, v83\n\t"
    "v_cvt_pk_bf16_f32 v168, v80, v81\n\t"
    "v_cvt_pk_bf16_f32 v169, v82, v83\n\t"
    "global_load_dwordx4 v[136:139], v176, s[44:45] offset:2048 sc1\n\t"
    "v_mfma_f32_32x32x16_bf16 v[64:79], v[184:187], v[116:119], v[64:79]\n\t"
    "v_exp_f32 v84, v84\n\t"
    "v_exp_f32 v85, v85\n\t"
    "v_exp_f32 v86, v86\n\t"
    "v_exp_f32 v87, v87\n\t"
    "v_cvt_pk_bf16_f32 v170, v84, v85\n\t"
    "v_cvt_pk_bf16_f32 v171, v86, v87\n\t"
    "global_load_dwordx4 v[140:143], v176, s[44:45] offset:3072 sc1\n\t"
    "s_waitcnt vmcnt(12)\n\t"
    "v_mfma_f32_32x32x16_bf16 v[0:15], v[188:191], v[160:163], v[0:15]\n\t"
    "v_exp_f32 v88, v88\n\t"
    "v_exp_f32 v89, v89\n\t"
    "v_exp_f32 v90, v90\n\t"
    "v_exp_f32 v91, v91\n\t"
    "v_cvt_pk_bf16_f32 v172, v88, v89\n\t"
    "v_cvt_pk_bf16_f32 v173, v90, v91\n\t"
    "v_mfma_f32_32x32x16_bf16 v[0:15], v[192:195], v[164:167], v[0:15]\n\t"
    "v_exp_f32 v92, v92\n\t"
    "v_exp_f32 v93, v93\n\t"
    "v_exp_f32 v94, v94\n\t"
    "v_exp_f32 v95, v95\n\t"
    "v_cvt_pk_bf16_f32 v174, v92, v93\n\t"
    "v_cvt_pk_bf16_f32 v175, v94, v95\n\t"
    "v_mfma_f32_32x32x16_bf16 v[80:95], v[180:183], v[120:123], 0\n\t"
    "v_exp_f32 v64, v64\n\t"
    "v_exp_f32 v65, v65\n\t"
    "v_exp_f32 v66, v66\n\t"
    "v_exp_f32 v67, v67\n\t"
    "v_cvt_pk_bf16_f32 v160, v64, v65\n\t"
    "v_cvt_pk_bf16_f32 v161, v66, v67\n\t"
    "global_load_dwordx4 v[144:147], v176, s[46:47] offset:0 sc1\n\t"
    "v_mfma_f32_32x32x16_bf16 v[80:95], v[184:187], v[124:127], v[80:95]\n\t"
    "v_exp_f32 v68, v68\n\t"
    "v_exp_f32 v69, v69\n\t"
    "v_exp_f32 v70, v70\n\t"
    "v_exp_f32 v71, v71\n\t"
    "v_cvt_pk_bf16_f32 v162, v68, v69\n\t"
    "v_cvt_pk_bf16_f32 v163, v70, v71\n\t"
    "global_load_dwordx4 v[148:151], v176, s[46:47] offset:1024 sc1\n\t"
    "v_mfma_f32_32x32x16_bf16 v[16:31], v[188:191], v[168:171], v[16:31]\n\t"
    "v_exp_f32 v72, v72\n\t"
    "v_exp_f32 v73, v73\n\t"
    "v_exp_f32 v74, v74\n\t"
    "v_exp_f32 v75, v75\n\t"
    "v_cvt_pk_bf16_f32 v164, v72, v73\n\t"
    "v_cvt_pk_bf16_f32 v165, v74, v75\n\t"
    "v_mfma_f32_32x32x16_bf16 v[16:31], v[192:195], v[172:175], v[16:31]\n\t"
    "v_exp_f32 v76, v76\n\t"
    "v_exp_f32 v77, v77\n\t"
    "v_exp_f32 v78, v78\n\t"
    "v_exp_f32 v79, v79\n\t"
    "v_cvt_pk_bf16_f32 v166, v76, v77\n\t"
    "v_cvt_pk_bf16_f32 v167, v78, v79\n\t"
    "s_waitcnt vmcnt(12)\n\t"
    "v_mfma_f32_32x32x16_bf16 v[64:79], v[196:199], v[96:99], 0\n\t"
    "v_exp_f32 v80, v80\n\t"
    "v_exp_f32 v81, v81\n\t"
    "v_exp_f32 v82, v82\n\t"
    "v_exp_f32 v83, v83\n\t"
    "v_cvt_pk_bf16_f32 v168, v80, v81\n\t"
    "v_cvt_pk_bf16_f32 v169, v82, v83\n\t"
    "global_load_dwordx4 v[152:155], v176, s[46:47] offset:2048 sc1\n\t"
    "v_mfma_f32_32x32x16_bf16 v[64:79], v[200:203], v[100:103], v[64:79]\n\t"
    "v_exp_f32 v84, v84\n\t"
    "v_exp_f32 v85, v85\n\t"
    "v_exp_f32 v86, v86\n\t"
    "v_exp_f32 v87, v87\n\t"
    "v_cvt_pk_bf16_f32 v170, v84, v85\n\t"
    "v_cvt_pk_bf16_f32 v171, v86, v87\n\t"
    "global_load_dwordx4 v[156:159], v176, s[46:47] offset:3072 sc1\n\t"
    "v_mfma_f32_32x32x16_bf16 v[32:47], v[188:191], v[160:163], v[32:47]\n\t"
    "v_exp_f32 v88, v88\n\t"
    "v_exp_f32 v89, v89\n\t"
    "v_exp_f32 v90, v90\n\t"
    "v_exp_f32 v91, v91\n\t"
    "v_cvt_pk_bf16_f32 v172, v88, v89\n\t"
    "v_cvt_pk_bf16_f32 v173, v90, v91\n\t"
    "v_mfma_f32_32x32x16_bf16 v[32:47], v[192:195], v[164:167], v[32:47]\n\t"
    "v_exp_f32 v92, v92\n\t"
    "v_exp_f32 v93, v93\n\t"
    "v_exp_f32 v94, v94\n\t"
    "v_exp_f32 v95, v95\n\t"
    "v_cvt_pk_bf16_f32 v174, v92, v93\n\t"
    "v_cvt_pk_bf16_f32 v175, v94, v95\n\t"
    "v_mfma_f32_32x32x16_bf16 v[80:95], v[196:199], v[104:107], 0\n\t"
    "v_exp_f32 v64, v64\n\t"
    "v_exp_f32 v65, v65\n\t"
    "v_exp_f32 v66, v66\n\t"
    "v_exp_f32 v67, v67\n\t"
    "v_cvt_pk_bf16_f32 v160, v64, v65\n\t"
    "v_cvt_pk_bf16_f32 v161, v66, v67\n\t"
    "s_mov_b32 m0, s42\n\t"
    "s_add_u32 s42, s42, s49\n\t"
    "global_load_lds_dwordx4 v177, s[40:41]\n\t"
    "s_add_u32 s40, s40, s48\n\t"
    "s_addc_u32 s41, s41, 0\n\t"
    "v_mfma_f32_32x32x16_bf16 v[80:95], v[200:203], v[108:111], v[80:95]\n\t"
    "v_exp_f32 v68, v68\n\t"
    "v_exp_f32 v69, v69\n\t"
    "v_exp_f32 v70, v70\n\t"
    "v_exp_f32 v71, v71\n\t"
    "v_cvt_pk_bf16_f32 v162, v68, v69\n\t"
    "v_cvt_pk_bf16_f32 v163, v70, v71\n\t"
    "v_mfma_f32_32x32x16_bf16 v[48:63], v[188:191], v[168:171], v[48:63]\n\t"
    "v_exp_f32 v72, v72\n\t"
    "v_exp_f32 v73, v73\n\t"
    "v_exp_f32 v74, v74\n\t"
    "v_exp_f32 v75, v75\n\t"
    "v_cvt_pk_bf16_f32 v164, v72, v73\n\t"
    "v_cvt_pk_bf16_f32 v165, v74, v75\n\t"
    "v_mfma_f32_32x32x16_bf16 v[48:63], v[192:195], v[172:175], v[48:63]\n\t"
    "v_exp_f32 v76, v76\n\t"
    "v_exp_f32 v77, v77\n\t"
    "v_exp_f32 v78, v78\n\t"
    "v_exp_f32 v79, v79\n\t"
    "v_cvt_pk_bf16_f32 v166, v76, v77\n\t"
    "v_cvt_pk_bf16_f32 v167, v78, v79\n\t"
    "v_mfma_f32_32x32x16_bf16 v[64:79], v[196:199], v[112:115], 0\n\t"
    "v_exp_f32 v80, v80\n\t"
    "v_exp_f32 v81, v81\n\t"
    "v_exp_f32 v82, v82\n\t"
    "v_exp_f32 v83, v83\n\t"
    "v_cvt_pk_bf16_f32 v168, v80, v81\n\t"
    "v_cvt_pk_bf16_f32 v169, v82, v83\n\t"
    "s_mov_b32 m0, s42\n\t"
    "s_add_u32 s42, s42, s49\n\t"
    "global_load_lds_dwordx4 v177, s[40:41]\n\t"
    "s_add_u32 s40, s40, s48\n\t"
    "s_addc_u32 s41, s41, 0\n\t"
    "v_mfma_f32_32x32x16_bf16 v[64:79], v[200:203], v[116:119], v[64:79]\n\t"
    "v_exp_f32 v84, v84\n\t"
    "v_exp_f32 v85, v85\n\t"
    "v_exp_f32 v86, v86\n\t"
    "v_exp_f32 v87, v87\n\t"
    "v_cvt_pk_bf16_f32 v170, v84, v85\n\t"
    "v_cvt_pk_bf16_f32 v171, v86, v87\n\t"
    "s_waitcnt vmcnt(14)\n\t"
    "v_mfma_f32_32x32x16_bf16 v[0:15], v[204:207], v[160:163], v[0:15]\n\t"
    "v_exp_f32 v88, v88\n\t"
    "v_exp_f32 v89, v89\n\t"
    "v_exp_f32 v90, v90\n\t"
    "v_exp_f32 v91, v91\n\t"
    "v_cvt_pk_bf16_f32 v172, v88, v89\n\t"
    "v_cvt_pk_bf16_f32 v173, v90, v91\n\t"
    "v_mfma_f32_32x32x16_bf16 v[0:15], v[208:211], v[164:167], v[0:15]\n\t"
    "v_exp_f32 v92, v92\n\t"
    "v_exp_f32 v93, v93\n\t"
    "v_exp_f32 v94, v94\n\t"
    "v_exp_f32 v95, v95\n\t"
    "v_cvt_pk_bf16_f32 v174, v92, v93\n\t"
    "v_cvt_pk_bf16_f32 v175, v94, v95\n\t"
    "v_mfma_f32_32x32x16_bf16 v[80:95], v[196:199], v[120:123], 0\n\t"
    "v_exp_f32 v64, v64\n\t"
    "v_exp_f32 v65, v65\n\t"
    "v_exp_f32 v66, v66\n\t"
    "v_exp_f32 v67, v67\n\t"
    "v_cvt_pk_bf16_f32 v160, v64, v65\n\t"
    "v_cvt_pk_bf16_f32 v161, v66, v67\n\t"
    "s_mov_b32 m0, s42\n\t"
    "s_add_u32 s42, s42, s49\n\t"
    "global_load_lds_dwordx4 v177, s[40:41]\n\t"
    "s_add_u32 s40, s40, s48\n\t"
    "s_addc_u32 s41, s41, 0\n\t"
    "v_mfma_f32_32x32x16_bf16 v[80:95], v[200:203], v[124:127], v[80:95]\n\t"
    "v_exp_f32 v68, v68\n\t"
    "v_exp_f32 v69, v69\n\t"
    "v_exp_f32 v70, v70\n\t"
    "v_exp_f32 v71, v71\n\t"
    "v_cvt_pk_bf16_f32 v162, v68, v69\n\t"
    "v_cvt_pk_bf16_f32 v163, v70, v71\n\t"
    "v_mfma_f32_32x32x16_bf16 v[16:31], v[204:207], v[168:171], v[16:31]\n\t"
    "v_exp_f32 v72, v72\n\t"
    "v_exp_f32 v73, v73\n\t"
    "v_exp_f32 v74, v74\n\t"
    "v_exp_f32 v75, v75\n\t"
    "v_cvt_pk_bf16_f32 v164, v72, v73\n\t"
    "v_cvt_pk_bf16_f32 v165, v74, v75\n\t"
    "v_mfma_f32_32x32x16_bf16 v[16:31], v[208:211], v[172:175], v[16:31]\n\t"
    "v_exp_f32 v76, v76\n\t"
    "v_exp_f32 v77, v77\n\t"
    "v_exp_f32 v78, v78\n\t"
    "v_exp_f32 v79, v79\n\t"
    "v_cvt_pk_bf16_f32 v166, v76, v77\n\t"
    "v_cvt_pk_bf16_f32 v167, v78, v79\n\t"
    "s_waitcnt vmcnt(9)\n\t"
    "v_mfma_f32_32x32x16_bf16 v[64:79], v[128:131], v[96:99], 0\n\t"
    "v_exp_f32 v80, v80\n\t"
    "v_exp_f32 v81, v81\n\t"
    "v_exp_f32 v82, v82\n\t"
    "v_exp_f32 v83, v83\n\t"
    "v_cvt_pk_bf16_f32 v168, v80, v81\n\t"
    "v_cvt_pk_bf16_f32 v169, v82, v83\n\t"
    "s_mov_b32 m0, s42\n\t"
    "s_add_u32 s42, s42, s49\n\t"
    "global_load_lds_dwordx4 v177, s[40:41]\n\t"
    "s_add_u32 s40, s40, s48\n\t"
    "s_addc_u32 s41, s41, 0\n\t"
    "v_mfma_f32_32x32x16_bf16 v[64:79], v[132:135], v[100:103], v[64:79]\n\t"
    "v_exp_f32 v84, v84\n\t"
    "v_exp_f32 v85, v85\n\t"
    "v_exp_f32 v86, v86\n\t"
    "v_exp_f32 v87, v87\n\t"
    "v_cvt_pk_bf16_f32 v170, v84, v85\n\t"
    "v_cvt_pk_bf16_f32 v171, v86, v87\n\t"
    "v_mfma_f32_32x32x16_bf16 v[32:47], v[204:207], v[160:163], v[32:47]\n\t"
    "v_exp_f32 v88, v88\n\t"
    "v_exp_f32 v89, v89\n\t"
    "v_exp_f32 v90, v90\n\t"
    "v_exp_f32 v91, v91\n\t"
    "v_cvt_pk_bf16_f32 v172, v88, v89\n\t"
    "v_cvt_pk_bf16_f32 v173, v90, v91\n\t"
    "v_mfma_f32_32x32x16_bf16 v[32:47], v[208:211], v[164:167], v[32:47]\n\t"
    "v_exp_f32 v92, v92\n\t"
    "v_exp_f32 v93, v93\n\t"
    "v_exp_f32 v94, v94\n\t"
    "v_exp_f32 v95, v95\n\t"
    "v_cvt_pk_bf16_f32 v174, v92, v93\n\t"
    "v_cvt_pk_bf16_f32 v175, v94, v95\n\t"
    "v_mfma_f32_32x32x16_bf16 v[48:63], v[204:207], v[168:171], v[48:63]\n\t"
    "s_nop 1\n\t"
    "v_mfma_f32_32x32x16_bf16 v[48:63], v[208:211], v[172:175], v[48:63]\n\t"
    "s_mov_b32 m0, s43\n\t"
    "s_nop 7\n\t"
    "s_nop 3"
        : ATT_IO : ATT_IN : ATT_CLOB);
    } else if (IDX == 2) {
        asm volatile(
    "s_mov_b32 s43, m0\n\t"
    "v_mfma_f32_32x32x16_bf16 v[80:95], v[128:131], v[104:107], 0\n\t"
    "v_exp_f32 v64, v64\n\t"
    "v_exp_f32 v65, v65\n\t"
    "v_exp_f32 v66, v66\n\t"
    "v_exp_f32 v67, v67\n\t"
    "v_cvt_pk_bf16_f32 v160, v64, v65\n\t"
    "v_cvt_pk_bf16_f32 v161, v66, v67\n\t"
    "global_load_dwordx4 v[180:183], v176, s[44:45] offset:0 sc1\n\t"
    "v_mfma_f32_32x32x16_bf16 v[80:95], v[132:135], v[108:111], v[80:95]\n\t"
    "v_exp_f32 v68, v68\n\t"
    "v_exp_f32 v69, v69\n\t"
    "v_exp_f32 v70, v70\n\t"
    "v_exp_f32 v71, v71\n\t"
    "v_cvt_pk_bf16_f32 v162, v68, v69\n\t"
    "v_cvt_pk_bf16_f32 v163, v70, v71\n\t"
    "global_load_dwordx4 v[184:187], v176, s[44:45] offset:1024 sc1\n\t"
    "v_exp_f32 v72, v72\n\t"
    "v_exp_f32 v73, v73\n\t"
    "v_exp_f32 v74, v74\n\t"
    "v_exp_f32 v75, v75\n\t"
    "v_cvt_pk_bf16_f32 v164, v72, v73\n\t"
    "v_cvt_pk_bf16_f32 v165, v74, v75\n\t"
    "v_exp_f32 v76, v76\n\t"
    "v_exp_f32 v77, v77\n\t"
    "v_exp_f32 v78, v78\n\t"
    "v_exp_f32 v79, v79\n\t"
    "v_cvt_pk_bf16_f32 v166, v76, v77\n\t"
    "v_cvt_pk_bf16_f32 v167, v78, v79\n\t"
    "v_mfma_f32_32x32x16_bf16 v[64:79], v[128:131], v[112:115], 0\n\t"
    "v_exp_f32 v80, v80\n\t"
    "v_exp_f32 v81, v81\n\t"
    "v_exp_f32 v82, v82\n\t"
    "v_exp_f32 v83, v83\n\t"
    "v_cvt_pk_bf16_f32 v168, v80, v81\n\t"
    "v_cvt_pk_bf16_f32 v169, v82, v83\n\t"
    "global_load_dwordx4 v[188:191], v176, s[44:45] offset:2048 sc1\n\t"
    "v_mfma_f32_32x32x16_bf16 v[64:79], v[132:135], v[116:119], v[64:79]\n\t"
    "v_exp_f32 v84, v84\n\t"
    "v_exp_f32 v85, v85\n\t"
    "v_exp_f32 v86, v86\n\t"
    "v_exp_f32 v87, v87\n\t"
    "v_cvt_pk_bf16_f32 v170, v84, v85\n\t"
    "v_cvt_pk_bf16_f32 v171, v86, v87\n\t"
    "global_load_dwordx4 v[192:195], v176, s[44:45] offset:3072 sc1\n\t"
    "s_waitcnt vmcnt(12)\n\t"
    "v_mfma_f32_32x32x16_bf16 v[0:15], v[136:139], v[160:163], v[0:15]\n\t"
    "v_exp_f32 v88, v88\n\t"
    "v_exp_f32 v89, v89\n\t"
    "v_exp_f32 v90, v90\n\t"
    "v_exp_f32 v91, v91\n\t"
    "v_cvt_pk_bf16_f32 v172, v88, v89\n\t"
    "v_cvt_pk_bf16_f32 v173, v90, v91\n\t"
    "v_mfma_f32_32x32x16_bf16 v[0:15], v[140:143], v[164:167], v[0:15]\n\t"
    "v_exp_f32 v92, v92\n\t"
    "v_exp_f32 v93, v93\n\t"
    "v_exp_f32 v94, v94\n\t"
    "v_exp_f32 v95, v95\n\t"
    "v_cvt_pk_bf16_f32 v174, v92, v93\n\t"
    "v_cvt_pk_bf16_f32 v175, v94, v95\n\t"
    "v_mfma_f32_32x32x16_bf16 v[80:95], v[128:131], v[120:123], 0\n\t"
    "v_exp_f32 v64, v64\n\t"
    "v_exp_f32 v65, v65\n\t"
    "v_exp_f32 v66, v66\n\t"
    "v_exp_f32 v67, v67\n\t"
    "v_cvt_pk_bf16_f32 v160, v64, v65\n\t"
    "v_cvt_pk_bf16_f32 v161, v66, v67\n\t"
    "global_load_dwordx4 v[196:199], v176, s[46:47] offset:0 sc1\n\t"
    "v_mfma_f32_32x32x16_bf16 v[80:95], v[132:135], v[124:127], v[80:95]\n\t"
    "v_exp_f32 v68, v68\n\t"
    "v_exp_f32 v69, v69\n\t"
    "v_exp_f32 v70, v70\n\t"
    "v_exp_f32 v71, v71\n\t"
    "v_cvt_pk_bf16_f32 v162, v68, v69\n\t"
    "v_cvt_pk_bf16_f32 v163, v70, v71\n\t"
    "global_load_dwordx4 v[200:203], v176, s[46:47] offset:1024 sc1\n\t"
    "v_mfma_f32_32x32x16_bf16 v[16:31], v[136:139], v[168:171], v[16:31]\n\t"
    "v_exp_f32 v72, v72\n\t"
    "v_exp_f32 v73, v73\n\t"
    "v_exp_f32 v74, v74\n\t"
    "v_exp_f32 v75, v75\n\t"
    "v_cvt_pk_bf16_f32 v164, v72, v73\n\t"
    "v_cvt_pk_bf16_f32 v165, v74, v75\n\t"
    "v_mfma_f32_32x32x16_bf16 v[16:31], v[140:143], v[172:175], v[16:31]\n\t"
    "v_exp_f32 v76, v76\n\t"
    "v_exp_f32 v77, v77\n\t"
    "v_exp_f32 v78, v78\n\t"
    "v_exp_f32 v79, v79\n\t"
    "v_cvt_pk_bf16_f32 v166, v76, v77\n\t"
    "v_cvt_pk_bf16_f32 v167, v78, v79\n\t"
    "s_waitcnt vmcnt(12)\n\t"
    "v_mfma_f32_32x32x16_bf16 v[64:79], v[144:147], v[96:99], 0\n\t"
    "v_exp_f32 v80, v80\n\t"
    "v_exp_f32 v81, v81\n\t"
    "v_exp_f32 v82, v82\n\t"
    "v_exp_f32 v83, v83\n\t"
    "v_cvt_pk_bf16_f32 v168, v80, v81\n\t"
    "v_cvt_pk_bf16_f32 v169, v82, v83\n\t"
    "global_load_dwordx4 v[204:207], v176, s[46:47] offset:2048 sc1\n\t"
    "v_mfma_f32_32x32x16_bf16 v[64:79], v[148:151], v[100:103], v[64:79]\n\t"
    "v_exp_f32 v84, v84\n\t"
    "v_exp_f32 v85, v85\n\t"
    "v_exp_f32 v86, v86\n\t"
    "v_exp_f32 v87, v87\n\t"
    "v_cvt_pk_bf16_f32 v170, v84, v85\n\t"
    "v_cvt_pk_bf16_f32 v171, v86, v87\n\t"
    "global_load_dwordx4 v[208:211], v176, s[46:47] offset:3072 sc1\n\t"
    "v_mfma_f32_32x32x16_bf16 v[32:47], v[136:139], v[160:163], v[32:47]\n\t"
    "v_exp_f32 v88, v88\n\t"
    "v_exp_f32 v89, v89\n\t"
    "v_exp_f32 v90, v90\n\t"
    "v_exp_f32 v91, v91\n\t"
    "v_cvt_pk_bf16_f32 v172, v88, v89\n\t"
    "v_cvt_pk_bf16_f32 v173, v90, v91\n\t"
    "v_mfma_f32_32x32x16_bf16 v[32:47], v[140:143], v[164:167], v[32:47]\n\t"
    "v_exp_f32 v92, v92\n\t"
    "v_exp_f32 v93, v93\n\t"
    "v_exp_f32 v94, v94\n\t"
    "v_exp_f32 v95, v95\n\t"
    "v_cvt_pk_bf16_f32 v174, v92, v93\n\t"
    "v_cvt_pk_bf16_f32 v175, v94, v95\n\t"
    "v_mfma_f32_32x32x16_bf16 v[80:95], v[144:147], v[104:107], 0\n\t"
    "v_exp_f32 v64, v64\n\t"
    "v_exp_f32 v65, v65\n\t"
    "v_exp_f32 v66, v66\n\t"
    "v_exp_f32 v67, v67\n\t"
    "v_cvt_pk_bf16_f32 v160, v64, v65\n\t"
    "v_cvt_pk_bf16_f32 v161, v66, v67\n\t"
    "s_mov_b32 m0, s42\n\t"
    "s_add_u32 s42, s42, s49\n\t"
    "global_load_lds_dwordx4 v177, s[40:41]\n\t"
    "s_add_u32 s40, s40, s48\n\t"
    "s_addc_u32 s41, s41, 0\n\t"
    "v_mfma_f32_32x32x16_bf16 v[80:95], v[148:151], v[108:111], v[80:95]\n\t"
    "v_exp_f32 v68, v68\n\t"
    "v_exp_f32 v69, v69\n\t"
    "v_exp_f32 v70, v70\n\t"
    "v_exp_f32 v71, v71\n\t"
    "v_cvt_pk_bf16_f32 v162, v68, v69\n\t"
    "v_cvt_pk_bf16_f32 v163, v70, v71\n\t"
    "v_mfma_f32_32x32x16_bf16 v[48:63], v[136:139], v[168:171], v[48:63]\n\t"
    "v_exp_f32 v72, v72\n\t"
    "v_exp_f32 v73, v73\n\t"
    "v_exp_f32 v74, v74\n\t"
    "v_exp_f32 v75, v75\n\t"
    "v_cvt_pk_bf16_f32 v164, v72, v73\n\t"
    "v_cvt_pk_bf16_f32 v165, v74, v75\n\t"
    "v_mfma_f32_32x32x16_bf16 v[48:63], v[140:143], v[172:175], v[48:63]\n\t"
    "v_exp_f32 v76, v76\n\t"
    "v_exp_f32 v77, v77\n\t"
    "v_exp_f32 v78, v78\n\t"
    "v_exp_f32 v79, v79\n\t"
    "v_cvt_pk_bf16_f32 v166, v76, v77\n\t"
    "v_cvt_pk_bf16_f32 v167, v78, v79\n\t"
    "v_mfma_f32_32x32x16_bf16 v[64:79], v[144:147], v[112:115], 0\n\t"
    "v_exp_f32 v80, v80\n\t"
    "v_exp_f32 v81, v81\n\t"
    "v_exp_f32 v82, v82\n\t"
    "v_exp_f32 v83, v83\n\t"
    "v_cvt_pk_bf16_f32 v168, v80, v81\n\t"
    "v_cvt_pk_bf16_f32 v169, v82, v83\n\t"
    "s_mov_b32 m0, s42\n\t"
    "s_add_u32 s42, s42, s49\n\t"
    "global_load_lds_dwordx4 v177, s[40:41]\n\t"
    "s_add_u32 s40, s40, s48\n\t"
    "s_addc_u32 s41, s41, 0\n\t"
    "v_mfma_f32_32x32x16_bf16 v[64:79], v[148:151], v[116:119], v[64:79]\n\t"
    "v_exp_f32 v84, v84\n\t"
    "v_exp_f32 v85, v85\n\t"
    "v_exp_f32 v86, v86\n\t"
    "v_exp_f32 v87, v87\n\t"
    "v_cvt_pk_bf16_f32 v170, v84, v85\n\t"
    "v_cvt_pk_bf16_f32 v171, v86, v87\n\t"
    "s_waitcnt vmcnt(14)\n\t"
    "v_mfma_f32_32x32x16_bf16 v[0:15], v[152:155], v[160:163], v[0:15]\n\t"
    "v_exp_f32 v88, v88\n\t"
    "v_exp_f32 v89, v89\n\t"
    "v_exp_f32 v90, v90\n\t"
    "v_exp_f32 v91, v91\n\t"
    "v_cvt_pk_bf16_f32 v172, v88, v89\n\t"
    "v_cvt_pk_bf16_f32 v173, v90, v91\n\t"
    "v_mfma_f32_32x32x16_bf16 v[0:15], v[156:159], v[164:167], v[0:15]\n\t"
    "v_exp_f32 v92, v92\n\t"
    "v_exp_f32 v93, v93\n\t"
    "v_exp_f32 v94, v94\n\t"
    "v_exp_f32 v95, v95\n\t"
    "v_cvt_pk_bf16_f32 v174, v92, v93\n\t"
    "v_cvt_pk_bf16_f32 v175, v94, v95\n\t"
    "v_mfma_f32_32x32x16_bf16 v[80:95], v[144:147], v[120:123], 0\n\t"
    "v_exp_f32 v64, v64\n\t"
    "v_exp_f32 v65, v65\n\t"
    "v_exp_f32 v66, v66\n\t"
    "v_exp_f32 v67, v67\n\t"
    "v_cvt_pk_bf16_f32 v160, v64, v65\n\t"
    "v_cvt_pk_bf16_f32 v161, v66, v67\n\t"
    "v_mfma_f32_32x32x16_bf16 v[80:95], v[148:151], v[124:127], v[80:95]\n\t"
    "v_exp_f32 v68, v68\n\t"
    "v_exp_f32 v69, v69\n\t"
    "v_exp_f32 v70, v70\n\t"
    "v_exp_f32 v71, v71\n\t"
    "v_cvt_pk_bf16_f32 v162, v68, v69\n\t"
    "v_cvt_pk_bf16_f32 v163, v70, v71\n\t"
    "v_mfma_f32_32x32x16_bf16 v[16:31], v[152:155], v[168:171], v[16:31]\n\t"
    "v_exp_f32 v72, v72\n\t"
    "v_exp_f32 v73, v73\n\t"
    "v_exp_f32 v74, v74\n\t"
    "v_exp_f32 v75, v75\n\t"
    "v_cvt_pk_bf16_f32 v164, v72, v73\n\t"
    "v_cvt_pk_bf16_f32 v165, v74, v75\n\t"
    "v_mfma_f32_32x32x16_bf16 v[16:31], v[156:159], v[172:175], v[16:31]\n\t"
    "v_exp_f32 v76, v76\n\t"
    "v_exp_f32 v77, v77\n\t"
    "v_exp_f32 v78, v78\n\t"
    "v_exp_f32 v79, v79\n\t"
    "v_cvt_pk_bf16_f32 v166, v76, v77\n\t"
    "v_cvt_pk_bf16_f32 v167, v78, v79\n\t"
    "s_waitcnt vmcnt(8)\n\t"
    "v_mfma_f32_32x32x16_bf16 v[64:79], v[180:183], v[96:99], 0\n\t"
    "v_exp_f32 v80, v80\n\t"
    "v_exp_f32 v81, v81\n\t"
    "v_exp_f32 v82, v82\n\t"
    "v_exp_f32 v83, v83\n\t"
    "v_cvt_pk_bf16_f32 v168, v80, v81\n\t"
    "v_cvt_pk_bf16_f32 v169, v82, v83\n\t"
    "v_mfma_f32_32x32x16_bf16 v[64:79], v[184:187], v[100:103], v[64:79]\n\t"
    "v_exp_f32 v84, v84\n\t"
    "v_exp_f32 v85, v85\n\t"
    "v_exp_f32 v86, v86\n\t"
    "v_exp_f32 v87, v87\n\t"
    "v_cvt_pk_bf16_f32 v170, v84, v85\n\t"
    "v_cvt_pk_bf16_f32 v171, v86, v87\n\t"
    "v_mfma_f32_32x32x16_bf16 v[32:47], v[152:155], v[160:163], v[32:47]\n\t"
    "v_exp_f32 v88, v88\n\t"
    "v_exp_f32 v89, v89\n\t"
    "v_exp_f32 v90, v90\n\t"
    "v_exp_f32 v91, v91\n\t"
    "v_cvt_pk_bf16_f32 v172, v88, v89\n\t"
    "v_cvt_pk_bf16_f32 v173, v90, v91\n\t"
    "v_mfma_f32_32x32x16_bf16 v[32:47], v[156:159], v[164:167], v[32:47]\n\t"
    "v_exp_f32 v92, v92\n\t"
    "v_exp_f32 v93, v93\n\t"
    "v_exp_f32 v94, v94\n\t"
    "v_exp_f32 v95, v95\n\t"
    "v_cvt_pk_bf16_f32 v174, v92, v93\n\t"
    "v_cvt_pk_bf16_f32 v175, v94, v95\n\t"
    "v_mfma_f32_32x32x16_bf16 v[48:63], v[152:155], v[168:171], v[48:63]\n\t"
    "s_nop 1\n\t"
    "v_mfma_f32_32x32x16_bf16 v[48:63], v[156:159], v[172:175], v[48:63]\n\t"
    "s_mov_b32 m0, s43\n\t"
    "s_nop 7\n\t"
    "s_nop 3"
        : ATT_IO : ATT_IN : ATT_CLOB);
    } else {
        asm volatile(
    "s_mov_b32 s43, m0\n\t"
    "v_mfma_f32_32x32x16_bf16 v[80:95], v[180:183], v[104:107], 0\n\t"
    "v_exp_f32 v64, v64\n\t"
    "v_exp_f32 v65, v65\n\t"
    "v_exp_f32 v66, v66\n\t"
    "v_exp_f32 v67, v67\n\t"
    "v_cvt_pk_bf16_f32 v160, v64, v65\n\t"
    "v_cvt_pk_bf16_f32 v161, v66, v67\n\t"
    "v_mfma_f32_32x32x16_bf16 v[80:95], v[184:187], v[108:111], v[80:95]\n\t"
    "v_exp_f32 v68, v68\n\t"
    "v_exp_f32 v69, v69\n\t"
    "v_exp_f32 v70, v70\n\t"
    "v_exp_f32 v71, v71\n\t"
    "v_cvt_pk_bf16_f32 v162, v68, v69\n\t"
    "v_cvt_pk_bf16_f32 v163, v70, v71\n\t"
    "v_exp_f32 v72, v72\n\t"
    "v_exp_f32 v73, v73\n\t"
    "v_exp_f32 v74, v74\n\t"
    "v_exp_f32 v75, v75\n\t"
    "v_cvt_pk_bf16_f32 v164, v72, v73\n\t"
    "v_cvt_pk_bf16_f32 v165, v74, v75\n\t"
    "v_exp_f32 v76, v76\n\t"
    "v_exp_f32 v77, v77\n\t"
    "v_exp_f32 v78, v78\n\t"
    "v_exp_f32 v79, v79\n\t"
    "v_cvt_pk_bf16_f32 v166, v76, v77\n\t"
    "v_cvt_pk_bf16_f32 v167, v78, v79\n\t"
    "v_mfma_f32_32x32x16_bf16 v[64:79], v[180:183], v[112:115], 0\n\t"
    "v_exp_f32 v80, v80\n\t"
    "v_exp_f32 v81, v81\n\t"
    "v_exp_f32 v82, v82\n\t"
    "v_exp_f32 v83, v83\n\t"
    "v_cvt_pk_bf16_f32 v168, v80, v81\n\t"
    "v_cvt_pk_bf16_f32 v169, v82, v83\n\t"
    "v_mfma_f32_32x32x16_bf16 v[64:79], v[184:187], v[116:119], v[64:79]\n\t"
    "v_exp_f32 v84, v84\n\t"
    "v_exp_f32 v85, v85\n\t"
    "v_exp_f32 v86, v86\n\t"
    "v_exp_f32 v87, v87\n\t"
    "v_cvt_pk_bf16_f32 v170, v84, v85\n\t"
    "v_cvt_pk_bf16_f32 v171, v86, v87\n\t"
    "s_waitcnt vmcnt(6)\n\t"
    "v_mfma_f32_32x32x16_bf16 v[0:15], v[188:191], v[160:163], v[0:15]\n\t"
    "v_exp_f32 v88, v88\n\t"
    "v_exp_f32 v89, v89\n\t"
    "v_exp_f32 v90, v90\n\t"
    "v_exp_f32 v91, v91\n\t"
    "v_cvt_pk_bf16_f32 v172, v88, v89\n\t"
    "v_cvt_pk_bf16_f32 v173, v90, v91\n\t"
    "v_mfma_f32_32x32x16_bf16 v[0:15], v[192:195], v[164:167], v[0:15]\n\t"
    "v_exp_f32 v92, v92\n\t"
    "v_exp_f32 v93, v93\n\t"
    "v_exp_f32 v94, v94\n\t"
    "v_exp_f32 v95, v95\n\t"
    "v_cvt_pk_bf16_f32 v174, v92, v93\n\t"
    "v_cvt_pk_bf16_f32 v175, v94, v95\n\t"
    "v_mfma_f32_32x32x16_bf16 v[80:95], v[180:183], v[120:123], 0\n\t"
    "v_exp_f32 v64, v64\n\t"
    "v_exp_f32 v65, v65\n\t"
    "v_exp_f32 v66, v66\n\t"
    "v_exp_f32 v67, v67\n\t"
    "v_cvt_pk_bf16_f32 v160, v64, v65\n\t"
    "v_cvt_pk_bf16_f32 v161, v66, v67\n\t"
    "v_mfma_f32_32x32x16_bf16 v[80:95], v[184:187], v[124:127], v[80:95]\n\t"
    "v_exp_f32 v68, v68\n\t"
    "v_exp_f32 v69, v69\n\t"
    "v_exp_f32 v70, v70\n\t"
    "v_exp_f32 v71, v71\n\t"
    "v_cvt_pk_bf16_f32 v162, v68, v69\n\t"
    "v_cvt_pk_bf16_f32 v163, v70, v71\n\t"
    "v_mfma_f32_32x32x16_bf16 v[16:31], v[188:191], v[168:171], v[16:31]\n\t"
    "v_exp_f32 v72, v72\n\t"
    "v_exp_f32 v73, v73\n\t"
    "v_exp_f32 v74, v74\n\t"
    "v_exp_f32 v75, v75\n\t"
    "v_cvt_pk_bf16_f32 v164, v72, v73\n\t"
    "v_cvt_pk_bf16_f32 v165, v74, v75\n\t"
    "v_mfma_f32_32x32x16_bf16 v[16:31], v[192:195], v[172:175], v[16:31]\n\t"
    "v_exp_f32 v76, v76\n\t"
    "v_exp_f32 v77, v77\n\t"
    "v_exp_f32 v78, v78\n\t"
    "v_exp_f32 v79, v79\n\t"
    "v_cvt_pk_bf16_f32 v166, v76, v77\n\t"
    "v_cvt_pk_bf16_f32 v167, v78, v79\n\t"
    "s_waitcnt vmcnt(4)\n\t"
    "v_mfma_f32_32x32x16_bf16 v[64:79], v[196:199], v[96:99], 0\n\t"
    "v_exp_f32 v80, v80\n\t"
    "v_exp_f32 v81, v81\n\t"
    "v_exp_f32 v82, v82\n\t"
    "v_exp_f32 v83, v83\n\t"
    "v_cvt_pk_bf16_f32 v168, v80, v81\n\t"
    "v_cvt_pk_bf16_f32 v169, v82, v83\n\t"
    "v_mfma_f32_32x32x16_bf16 v[64:79], v[200:203], v[100:103], v[64:79]\n\t"
    "v_exp_f32 v84, v84\n\t"
    "v_exp_f32 v85, v85\n\t"
    "v_exp_f32 v86, v86\n\t"
    "v_exp_f32 v87, v87\n\t"
    "v_cvt_pk_bf16_f32 v170, v84, v85\n\t"
    "v_cvt_pk_bf16_f32 v171, v86, v87\n\t"
    "v_mfma_f32_32x32x16_bf16 v[32:47], v[188:191], v[160:163], v[32:47]\n\t"
    "v_exp_f32 v88, v88\n\t"
    "v_exp_f32 v89, v89\n\t"
    "v_exp_f32 v90, v90\n\t"
    "v_exp_f32 v91, v91\n\t"
    "v_cvt_pk_bf16_f32 v172, v88, v89\n\t"
    "v_cvt_pk_bf16_f32 v173, v90, v91\n\t"
    "v_mfma_f32_32x32x16_bf16 v[32:47], v[192:195], v[164:167], v[32:47]\n\t"
    "v_exp_f32 v92, v92\n\t"
    "v_exp_f32 v93, v93\n\t"
    "v_exp_f32 v94, v94\n\t"
    "v_exp_f32 v95, v95\n\t"
    "v_cvt_pk_bf16_f32 v174, v92, v93\n\t"
    "v_cvt_pk_bf16_f32 v175, v94, v95\n\t"
    "v_mfma_f32_32x32x16_bf16 v[80:95], v[196:199], v[104:107], 0\n\t"
    "v_exp_f32 v64, v64\n\t"
    "v_exp_f32 v65, v65\n\t"
    "v_exp_f32 v66, v66\n\t"
    "v_exp_f32 v67, v67\n\t"
    "v_cvt_pk_bf16_f32 v160, v64, v65\n\t"
    "v_cvt_pk_bf16_f32 v161, v66, v67\n\t"
    "s_mov_b32 m0, s42\n\t"
    "s_add_u32 s42, s42, s49\n\t"
    "global_load_lds_dwordx4 v177, s[40:41]\n\t"
    "s_add_u32 s40, s40, s48\n\t"
    "s_addc_u32 s41, s41, 0\n\t"
    "v_mfma_f32_32x32x16_bf16 v[80:95], v[200:203], v[108:111], v[80:95]\n\t"
    "v_exp_f32 v68, v68\n\t"
    "v_exp_f32 v69, v69\n\t"
    "v_exp_f32 v70, v70\n\t"
    "v_exp_f32 v71, v71\n\t"
    "v_cvt_pk_bf16_f32 v162, v68, v69\n\t"
    "v_cvt_pk_bf16_f32 v163, v70, v71\n\t"
    "v_mfma_f32_32x32x16_bf16 v[48:63], v[188:191], v[168:171], v[48:63]\n\t"
    "v_exp_f32 v72, v72\n\t"
    "v_exp_f32 v73, v73\n\t"
    "v_exp_f32 v74, v74\n\t"
    "v_exp_f32 v75, v75\n\t"
    "v_cvt_pk_bf16_f32 v164, v72, v73\n\t"
    "v_cvt_pk_bf16_f32 v165, v74, v75\n\t"
    "v_mfma_f32_32x32x16_bf16 v[48:63], v[192:195], v[172:175], v[48:63]\n\t"
    "v_exp_f32 v76, v76\n\t"
    "v_exp_f32 v77, v77\n\t"
    "v_exp_f32 v78, v78\n\t"
    "v_exp_f32 v79, v79\n\t"
    "v_cvt_pk_bf16_f32 v166, v76, v77\n\t"
    "v_cvt_pk_bf16_f32 v167, v78, v79\n\t"
    "v_mfma_f32_32x32x16_bf16 v[64:79], v[196:199], v[112:115], 0\n\t"
    "v_exp_f32 v80, v80\n\t"
    "v_exp_f32 v81, v81\n\t"
    "v_exp_f32 v82, v82\n\t"
    "v_exp_f32 v83, v83\n\t"
    "v_cvt_pk_bf16_f32 v168, v80, v81\n\t"
    "v_cvt_pk_bf16_f32 v169, v82, v83\n\t"
    "s_mov_b32 m0, s42\n\t"
    "s_add_u32 s42, s42, s49\n\t"
    "global_load_lds_dwordx4 v177, s[40:41]\n\t"
    "s_add_u32 s40, s40, s48\n\t"
    "s_addc_u32 s41, s41, 0\n\t"
    "v_mfma_f32_32x32x16_bf16 v[64:79], v[200:203], v[116:119], v[64:79]\n\t"
    "v_exp_f32 v84, v84\n\t"
    "v_exp_f32 v85, v85\n\t"
    "v_exp_f32 v86, v86\n\t"
    "v_exp_f32 v87, v87\n\t"
    "v_cvt_pk_bf16_f32 v170, v84, v85\n\t"
    "v_cvt_pk_bf16_f32 v171, v86, v87\n\t"
    "s_waitcnt vmcnt(4)\n\t"
    "v_mfma_f32_32x32x16_bf16 v[0:15], v[204:207], v[160:163], v[0:15]\n\t"
    "v_exp_f32 v88, v88\n\t"
    "v_exp_f32 v89, v89\n\t"
    "v_exp_f32 v90, v90\n\t"
    "v_exp_f32 v91, v91\n\t"
    "v_cvt_pk_bf16_f32 v172, v88, v89\n\t"
    "v_cvt_pk_bf16_f32 v173, v90, v91\n\t"
    "v_mfma_f32_32x32x16_bf16 v[0:15], v[208:211], v[164:167], v[0:15]\n\t"
    "v_exp_f32 v92, v92\n\t"
    "v_exp_f32 v93, v93\n\t"
    "v_exp_f32 v94, v94\n\t"
    "v_exp_f32 v95, v95\n\t"
    "v_cvt_pk_bf16_f32 v174, v92, v93\n\t"
    "v_cvt_pk_bf16_f32 v175, v94, v95\n\t"
    "v_mfma_f32_32x32x16_bf16 v[80:95], v[196:199], v[120:123], 0\n\t"
    "v_exp_f32 v64, v64\n\t"
    "v_exp_f32 v65, v65\n\t"
    "v_exp_f32 v66, v66\n\t"
    "v_exp_f32 v67, v67\n\t"
    "v_cvt_pk_bf16_f32 v160, v64, v65\n\t"
    "v_cvt_pk_bf16_f32 v161, v66, v67\n\t"
    "v_mfma_f32_32x32x16_bf16 v[80:95], v[200:203], v[124:127], v[80:95]\n\t"
    "v_exp_f32 v68, v68\n\t"
    "v_exp_f32 v69, v69\n\t"
    "v_exp_f32 v70, v70\n\t"
    "v_exp_f32 v71, v71\n\t"
    "v_cvt_pk_bf16_f32 v162, v68, v69\n\t"
    "v_cvt_pk_bf16_f32 v163, v70, v71\n\t"
    "v_mfma_f32_32x32x16_bf16 v[16:31], v[204:207], v[168:171], v[16:31]\n\t"
    "v_exp_f32 v72, v72\n\t"
    "v_exp_f32 v73, v73\n\t"
    "v_exp_f32 v74, v74\n\t"
    "v_exp_f32 v75, v75\n\t"
    "v_cvt_pk_bf16_f32 v164, v72, v73\n\t"
    "v_cvt_pk_bf16_f32 v165, v74, v75\n\t"
    "v_mfma_f32_32x32x16_bf16 v[16:31], v[208:211], v[172:175], v[16:31]\n\t"
    "v_exp_f32 v76, v76\n\t"
    "v_exp_f32 v77, v77\n\t"
    "v_exp_f32 v78, v78\n\t"
    "v_exp_f32 v79, v79\n\t"
    "v_cvt_pk_bf16_f32 v166, v76, v77\n\t"
    "v_cvt_pk_bf16_f32 v167, v78, v79\n\t"
    "v_exp_f32 v80, v80\n\t"
    "v_exp_f32 v81, v81\n\t"
    "v_exp_f32 v82, v82\n\t"
    "v_exp_f32 v83, v83\n\t"
    "v_cvt_pk_bf16_f32 v168, v80, v81\n\t"
    "v_cvt_pk_bf16_f32 v169, v82, v83\n\t"
    "v_exp_f32 v84, v84\n\t"
    "v_exp_f32 v85, v85\n\t"
    "v_exp_f32 v86, v86\n\t"
    "v_exp_f32 v87, v87\n\t"
    "v_cvt_pk_bf16_f32 v170, v84, v85\n\t"
    "v_cvt_pk_bf16_f32 v171, v86, v87\n\t"
    "v_mfma_f32_32x32x16_bf16 v[32:47], v[204:207], v[160:163], v[32:47]\n\t"
    "v_exp_f32 v88, v88\n\t"
    "v_exp_f32 v89, v89\n\t"
    "v_exp_f32 v90, v90\n\t"
    "v_exp_f32 v91, v91\n\t"
    "v_cvt_pk_bf16_f32 v172, v88, v89\n\t"
    "v_cvt_pk_bf16_f32 v173, v90, v91\n\t"
    "v_mfma_f32_32x32x16_bf16 v[32:47], v[208:211], v[164:167], v[32:47]\n\t"
    "v_exp_f32 v92, v92\n\t"
    "v_exp_f32 v93, v93\n\t"
    "v_exp_f32 v94, v94\n\t"
    "v_exp_f32 v95, v95\n\t"
    "v_cvt_pk_bf16_f32 v174, v92, v93\n\t"
    "v_cvt_pk_bf16_f32 v175, v94, v95\n\t"
    "v_mfma_f32_32x32x16_bf16 v[48:63], v[204:207], v[168:171], v[48:63]\n\t"
    "s_nop 1\n\t"
    "v_mfma_f32_32x32x16_bf16 v[48:63], v[208:211], v[172:175], v[48:63]\n\t"
    "s_mov_b32 m0, s43\n\t"
    "s_nop 7\n\t"
    "s_nop 3"
        : ATT_IO : ATT_IN : ATT_CLOB);
    }
}
DI void rescale(f32x16& o, float& mref, bf16x8& q1, int hh, float& dout) {
    const float l = swap_hi(o[12]);
    const float mn = l > 65536.f ? mref + __builtin_amdgcn_logf(l) : mref;
    const unsigned mb = pk(-mn, 0.f) & 0xffffu; const float m2 = -__uint_as_float(mb << 16), d = m2 - mref, f = __builtin_amdgcn_exp2f(-d);
    mref = m2; if (hh) q1[4] = (short)mb;
#pragma unroll
    for (int i = 0; i < 16; ++i) o[i] *= f;
    dout = d;
}
DI void attn_safe(rsrc_t rs, unsigned kv, int lane, bf16x8 q0, bf16x8 q1, f32x16& o, float& mref) {
    const int hh = lane >> 5; const f32x16 zero = {};
    if (hh) q1[4] = 0;
    float m = -INFINITY; o = zero;
    for (int tt = 0; tt < 8; ++tt) {
        const unsigned kt = kv + tt * 4096 + lane * 16;
        f32x16 x = MFMA(ld_dev(rs, kt), q0, zero); x = MFMA(ld_dev(rs, kt + 1024), q1, x);
        const float mn = __builtin_fmaxf(m, halfmax(max16(x))), f = __builtin_amdgcn_exp2f(m - mn);
#pragma unroll
        for (int i = 0; i < 16; ++i) { x[i] = __builtin_amdgcn_exp2f(x[i] - mn); o[i] *= f; }
        m = mn;
        o = MFMA(ld_dev(rs, kt + 2048), pack8(x, 0), o); o = MFMA(ld_dev(rs, kt + 3072), pack8(x, 1), o);
    }
    mref = m;
}
DI bool o_bad(const f32x16& o, int hh) {
    f32x2 a = {o[0], o[1]}, b = {o[2], o[3]}, c = {o[4], o[5]}, d = {o[6], o[7]};
    a += (f32x2){o[8], o[9]}; b += (f32x2){o[10], o[11]}; c += (f32x2){o[12], o[13]}; d += (f32x2){o[14], o[15]};
    a += b; c += d; a += c;
    const float l = o[12];
    const bool fin = __builtin_fabsf(a[0] + a[1]) < 3.0e38f, lok = (l > 7.5e-37f) & (l < 1.3e36f);
    return (!fin) | ((hh != 0) & (!lok));
}

__global__ __launch_bounds__(512, 2) void k_fused(Params p) {
    __shared__ __attribute__((aligned(16))) char ldsg[LDS_BYTES];
    const int tid = threadIdx.x, lane = tid & 63, w = __builtin_amdgcn_readfirstlane(tid >> 6), b = blockIdx.x;
    const int xcd = b & 7, j = b >> 3;
    { u64 pc; asm volatile("s_getpc_b64 %0" : "=s"(pc));
      if (w == 3 && j < 8) touch((const char*)(pc & ~127ull) + (j * 64 + lane < 448 ? j * 64 + lane : 0) * 128, (unsigned)(uintptr_t)ldsg + L_TOUCH); }
    const int r = lane & 31, hh = lane >> 5;
    lptr lds = (lptr)ldsg; const unsigned lds0 = (unsigned)(uintptr_t)ldsg;
    const rsrc_t rs = ws_rsrc(p.ws);
    const unsigned vo = lane * 16;
    const int h = 2 * xcd + (j >> 4), qb = j & 15;
    const unsigned kvh = WS_KV + h * 64 * 4096, kvo = kvh + w * 8 * 4096;
    const char* kvw = p.ws + kvo;
    bf16x8 kv[16]; unsigned tk_keep = 0; bf16x8 pb[6];
    const bool has_tile = j < 28;
    const int ot = has_tile ? j : 0, og = xcd * 4 + ot / 7, ocg = ot % 7, orb = og * 2 + (w & 1);
    const int mt = (xcd & 3) * 4 + (j & 3), g = (xcd >> 2) * 8 + (j >> 2);
    u64 tag;
#define READ_TAG() tag = flag_get(p.ws, F_GEN) * 6364136223846793005ull + 1442695040888963407ull
    if (__builtin_expect(w >= 4, 1)) {
        const int i4 = w - 4, cb = (168 * g) & ~15;
        unsigned voff[7];
        const char* xga = p.xa + (size_t)mt * 128 * XROW;
#pragma unroll
        for (int i = 0; i < 4; ++i) { const int rr = i4 * 32 + 8 * i + (lane >> 3); voff[i] = rr * XROW + (((lane & 7) ^ ((rr >> 1) & 7)) << 4); glds(xga, voff[i], lds0 + i4 * 4096 + i * 1024); }
        const char* wga = p.wa + cb;
#pragma unroll
        for (int i = 0; i < 3; ++i) { const int n = (i4 * 3 + i) * 64 + lane, row = (n * 5462) >> 16, c = n - row * 12; voff[4 + i] = row * WROW + (c < 11 ? c : 10) * 16;
            glds(wga, voff[4 + i], lds0 + G_XB + i4 * 3072 + i * 1024); }
        const char* xgb = p.xb + (size_t)mt * 128 * XROW; const char* wgb = p.wb + cb;
#define G_ISSUE(c) do { const char* xb_ = ((c) < 7 ? xga : xgb) + ((c) % 7) * 128; const char* wb_ = ((c) < 7 ? wga : wgb) + (size_t)((c) % 7) * 64 * WROW; \
        const unsigned l_ = lds0 + ((c) % G_NST) * G_STAGE; \
        glds(xb_, voff[0], l_ + i4 * 4096); glds(xb_, voff[1], l_ + i4 * 4096 + 1024); glds(xb_, voff[2], l_ + i4 * 4096 + 2048); glds(xb_, voff[3], l_ + i4 * 4096 + 3072); \
        glds(wb_, voff[4], l_ + G_XB + i4 * 3072); glds(wb_, voff[5], l_ + G_XB + i4 * 3072 + 1024); glds(wb_, voff[6], l_ + G_XB + i4 * 3072 + 2048); } while (0)
#define G_LSTEP(c, AHEAD) do { wait_vm<7 * (AHEAD)>(); bar(); if ((c) + 3 < 14) G_ISSUE((c) + 3); } while (0)
        wait_vm<0>(); bar(); G_ISSUE(1); G_ISSUE(2); G_ISSUE(3);
        G_LSTEP(1, 2); G_LSTEP(2, 2); G_LSTEP(3, 2); READ_TAG();
        { const int m0 = (xcd & 3) * 512, c0 = ((xcd >> 2) * 1344) & ~127;
          { const int idx = i4 * 2048 + j * 64 + lane; const char* a_;
            if (idx < 4928) { const int row = idx / 11; a_ = p.wb + (size_t)row * WROW + c0 + (idx - row * 11) * 128; }
            else { const int i = idx - 4928, row = i / 7; a_ = p.xb + (size_t)(m0 + row) * XROW + (i - row * 7) * 128; }
            touch(a_, lds0 + L_TOUCH); }
          if (i4 == 0 && j < 5) { const int i = 8192 - 4928 + j * 64 + lane, row = i / 7; touch(p.xb + (size_t)(m0 + (row < 512 ? row : 511)) * XROW + (i - row * 7) * 128, lds0 + L_TOUCH); } }
         G_LSTEP(4, 2); G_LSTEP(5, 2); G_LSTEP(6, 2);
        G_LSTEP(7, 2); G_LSTEP(8, 2); G_LSTEP(9, 2); G_LSTEP(10, 2);
        wait_vm<0>(); bar();
        flags_wait(p.ws, F_A + (lane < 16 ? ((16 + h) / 3) * 128 + lane * 8 : lane < 32 ? ((32 + h) / 3) * 128 + (lane - 16) * 8 : (h / 3) * 128 + qb * 8), tag);
        const unsigned qs = WS_Q + ((h * 64 + qb * 4) * 2 + i4 * 2) * 1024 + vo;
        const bf16x8 t0 = ld_dev(rs, qs), t1 = ld_dev(rs, qs + 1024), t2 = ld_dev(rs, kvh + vo), t3 = ld_dev(rs, kvh + 1024 + vo);
#pragma unroll
        for (int i = 0; i < 8; ++i) kv[i] = ld_dev(rs, kvo + i * 1024 + vo);
        unsigned tk = 0;
        if (i4 < 2) tk = __builtin_amdgcn_raw_buffer_load_b32(rs, kvh + (qb * 128 + i4 * 64 + lane) * 128, 0, 16);
        { const f32x16 zero = {};
          f32x16 s = MFMA(t2, t0, zero); s = MFMA(t3, t1, s);
          *(LAS bf16x8*)(lds + L_Q + i4 * 2048 + vo) = t0; *(LAS bf16x8*)(lds + L_Q + i4 * 2048 + 1024 + vo) = t1;
          const float m = halfmax(max16(s));
          if (!hh) *(LAS unsigned*)(lds + L_M0 + i4 * 128 + r * 4) = pk(-m, 0.f) & 0xffffu; }
        bar();
        tk_keep = tk;
#undef G_ISSUE
#undef G_LSTEP
    } else {
        const int blk = (lane >> 4) & 1, q = (lane & 15) >> 2, pp = lane & 3;
        const int xrow = (w * 32 + r) * 128, cx = hh ^ ((r >> 1) & 7);
        const int wl = G_XB + (8 * hh + q) * 192 + ((168 * g) & 15) + 32 * blk + 8 * pp;
        const int rb = mt * 4 + w;
        f32x16 a0 = {}, a1 = {}, a2 = {};
        if (tid < 2) *(LAS u32x4*)(lds + L_CNT + tid * 16) = (u32x4){0u, 0u, 0u, 0u};
        const unsigned tl = lds0 + L_TOUCH;
        const int m0 = (xcd & 3) * 512, c0 = ((xcd >> 2) * 1344) & ~127;
        { const int idx = w * 2048 + j * 64 + lane; const char* a;
          if (idx < 3072) { const int row = idx / 6; a = p.xa + (size_t)(m0 + row) * XROW + (1 + idx - row * 6) * 128; }
          else if (idx < 7296) { const int i = idx - 3072, row = i / 11; a = p.wa + (size_t)(64 + row) * WROW + c0 + (i - row * 11) * 128; }
          else a = p.xa;
          touch(a, tl); }
#define TOUCH_O() do { const int idx = w * 2048 + j * 64 + lane; \
          const char* a = idx < 3136 ? p.wob + (size_t)idx * 128 : idx < 6272 ? p.woa + (size_t)(idx - 3136) * 128 : p.woa; \
          touch(a, tl); } while (0)
        bf16x8 pa[6];
#define G_STEP(c, V2, V01) do { bar(); lptr s_ = lds + ((c) % G_NST) * G_STAGE; g_chunk<V2, V01>(s_ + xrow, cx, s_ + wl, a0, a1, a2); } while (0)
#define G_NOBAR(c, V2, V01) do { lptr s_ = lds + ((c) % G_NST) * G_STAGE; g_chunk<V2, V01>(s_ + xrow, cx, s_ + wl, a0, a1, a2); } while (0)
#define A_SIGNAL(MUST) do { if (!a_done && ((MUST) || (__builtin_amdgcn_s_getreg(7 | (31 << 11)) & 0x00C0000Fu) == 0u)) { a_done = true; wait_vm<0>(); \
        if (lane == 0 && __hip_atomic_fetch_add((LAS unsigned*)(lds + L_CNT + 16), 1u, __ATOMIC_RELAXED, __HIP_MEMORY_SCOPE_WORKGROUP) == 3u) flag_set(p.ws, F_A + g * 128 + mt * 8, tag); } } while (0)
#define G_LOOP(V2, V01) do { \
        G_STEP(0, V2, V01); G_STEP(1, V2, V01); G_STEP(2, V2, V01); G_STEP(3, V2, V01); READ_TAG(); G_STEP(4, V2, V01); G_STEP(5, V2, V01); G_STEP(6, V2, V01); \
        g_pack(a0, 3 * g, lane, QS, pa[0], pa[1]); g_pack(a1, 3 * g + 1, lane, QS, pa[2], pa[3]); g_pack(a2, 3 * g + 2, lane, QS, pa[4], pa[5]); \
        a0 = (f32x16){}; a1 = (f32x16){}; a2 = (f32x16){}; \
        g_put(rs, 0u, 3 * g, rb, lane, pa[0], pa[1]); g_put(rs, 0u, 3 * g + 1, rb, lane, pa[2], pa[3]); g_put(rs, 0u, 3 * g + 2, rb, lane, pa[4], pa[5]); \
        G_STEP(7, V2, V01); A_SIGNAL(false); G_STEP(8, V2, V01); A_SIGNAL(false); G_STEP(9, V2, V01); A_SIGNAL(false); G_STEP(10, V2, V01); A_SIGNAL(true); \
        G_STEP(11, V2, V01); TOUCH_O(); G_NOBAR(12, V2, V01); G_NOBAR(13, V2, V01); } while (0)
        bool a_done = false;
        if (g >= 11) G_LOOP(true, true); else if (g == 10) G_LOOP(true, false); else G_LOOP(false, false);
        g_pack(a0, 3 * g, lane, 1.f, pb[0], pb[1]); g_pack(a1, 3 * g + 1, lane, 1.f, pb[2], pb[3]); g_pack(a2, 3 * g + 2, lane, 1.f, pb[4], pb[5]);
#undef G_STEP
#undef G_NOBAR
#undef A_SIGNAL
#undef G_LOOP
        flags_wait(p.ws, F_A + (lane < 16 ? ((16 + h) / 3) * 128 + lane * 8 : ((32 + h) / 3) * 128 + (lane & 15) * 8), tag);
#pragma unroll
        for (int i = 0; i < 8; ++i) kv[i] = ld_dev(rs, kvo + i * 1024 + vo);
        bar();
    }
    const f32x16 zero = {};
    bf16x8 q[8];
#pragma unroll
    for (int i = 0; i < 8; ++i) q[i] = lds16(lds + L_Q + i * 1024 + vo);
    float mr0, mr1, mr2, mr3;
    { const unsigned b0 = *(LAS unsigned*)(lds + L_M0 + r * 4), b1 = *(LAS unsigned*)(lds + L_M0 + 128 + r * 4), b2 = *(LAS unsigned*)(lds + L_M0 + 256 + r * 4), b3 = *(LAS unsigned*)(lds + L_M0 + 384 + r * 4);
      mr0 = -__uint_as_float(b0 << 16); mr1 = -__uint_as_float(b1 << 16); mr2 = -__uint_as_float(b2 << 16); mr3 = -__uint_as_float(b3 << 16);
      if (hh) { q[1][4] = (short)b0; q[3][4] = (short)b1; q[5][4] = (short)b2; q[7][4] = (short)b3; } }
    asm volatile("s_waitcnt lgkmcnt(0)" ::: "memory");
    if (lane == 0) __hip_atomic_fetch_add((LAS unsigned*)(lds + L_CNT + 12), 1u, __ATOMIC_RELAXED, __HIP_MEMORY_SCOPE_WORKGROUP);
    if (w < 4) {
        const int rb = mt * 4 + w;
        g_put(rs, WS_BR, 3 * g, rb, lane, pb[0], pb[1]); g_put(rs, WS_BR, 3 * g + 1, rb, lane, pb[2], pb[3]); g_put(rs, WS_BR, 3 * g + 2, rb, lane, pb[4], pb[5]);
    }
    asm volatile("" :: "v"(tk_keep));
    f32x16 sa = MFMA(kv[0], q[0], zero); sa = MFMA(kv[1], q[1], sa);
#pragma unroll
    for (int i = 8; i < 16; ++i) kv[i] = kv[0];
    f32x16 o0 = {}, o1 = {}, o2 = {}, o3 = {};
    const int wb7 = w < 7 ? w : 0;
    const unsigned wvoff = (lane >> 3) * XROW + (((lane & 7) ^ (((lane >> 4) & 1) << 2)) << 4);
    const char* wp = p.wob + ocg * 128 + (size_t)wb7 * 8 * XROW; unsigned wl = lds0 + L_WB + wb7 * 1024;
    const char* wq = p.woa + ocg * 128 + (size_t)wb7 * 32 * XROW; unsigned wm = lds0 + L_WA + wb7 * 4096;
#define RESCALE_CHECK() do { if (__any(__builtin_fmaxf(__builtin_fmaxf(__builtin_fabsf(o0[12]), __builtin_fabsf(o1[12])), __builtin_fmaxf(__builtin_fabsf(o2[12]), __builtin_fabsf(o3[12]))) > 1.2e24f)) { \
            float d, dd; rescale(o0, mr0, q[1], hh, d); rescale(o1, mr1, q[3], hh, dd); rescale(o2, mr2, q[5], hh, dd); rescale(o3, mr3, q[7], hh, dd); \
            _Pragma("unroll") for (int i = 0; i < 16; ++i) sa[i] -= d; } } while (0)
    __builtin_amdgcn_s_setprio(3);
    attn_block<0>(o0, o1, o2, o3, sa, q, kv, vo, wvoff, wp, wl, 56 * XROW, 7168, kvw + 2 * 4096, kvw + 3 * 4096);
    if (w < 4) {
        wait_vm<12>();
        if (lane == 0 && __hip_atomic_fetch_add((LAS unsigned*)(lds + L_CNT + 8), 1u, __ATOMIC_RELAXED, __HIP_MEMORY_SCOPE_WORKGROUP) == 3u) flag_set(p.ws, F_B + g * 128 + mt * 8, tag);
    }
    __builtin_amdgcn_s_setprio(2);
    RESCALE_CHECK();
    attn_block<1>(o0, o1, o2, o3, sa, q, kv, vo, wvoff, wp, wl, 56 * XROW, 7168, kvw + 4 * 4096, kvw + 5 * 4096);
    RESCALE_CHECK();
    if (w < 4) {
        const int hb = b & 15, gb = b >> 4, gp = gb > 0 ? gb - 1 : 0;
        flags_wait(p.ws, F_B + (lane == 0 ? (hb / 3) * 128 + gb * 8 : lane == 1 ? ((16 + hb) / 3) * 128 + gb * 8 : lane == 2 ? ((32 + hb) / 3) * 128 + gb * 8
                                : lane == 3 ? ((16 + hb) / 3) * 128 + gp * 8 : ((32 + hb) / 3) * 128 + gp * 8), tag);
        local_unit(rs, gb * 4 + w, hb, lane);
        wait_vm<0>();
        if (lane == 0 && __hip_atomic_fetch_add((LAS unsigned*)(lds + L_CNT), 1u, __ATOMIC_RELAXED, __HIP_MEMORY_SCOPE_WORKGROUP) == 3u) flag_set(p.ws, F_OB + gb * 128 + hb * 8, tag);
    }
    if (b == 0 && w == 7) {
#pragma unroll 1
        for (int i = 0; i < 4; ++i) flags_wait(p.ws, F_A + (i * 64 + lane) * 8, tag);
        if (lane == 0) flag_set(p.ws, F_GEN, tag);
    }
    __builtin_amdgcn_s_setprio(1);
    attn_block<2>(o0, o1, o2, o3, sa, q, kv, vo, wvoff, wq, wm, 8 * XROW, 1024, kvw + 6 * 4096, kvw + 7 * 4096);
    __builtin_amdgcn_s_setprio(0);
    RESCALE_CHECK();
    const unsigned fob = F_OB + (og >> 1) * 128 + (lane & 15) * 8;
    const u64 fob_seen = flag_get(p.ws, fob);
    attn_block<3>(o0, o1, o2, o3, sa, q, kv, vo, wvoff, wq, wm, 8 * XROW, 1024, kvw, kvw);
    u32x4 ob[8];
    if (has_tile) {
        if (!__all(fob_seen == tag)) flags_wait(p.ws, fob, tag);
#pragma unroll
        for (int i = 0; i < 8; ++i) ob[i] = __builtin_amdgcn_raw_buffer_load_b128(rs, WS_O + WS_OBR + og * 65536 + w * 8192 + i * 1024 + vo, 0, 16);
    }
    { const bool b0 = o_bad(o0, hh), b1 = o_bad(o1, hh), b2 = o_bad(o2, hh), b3 = o_bad(o3, hh);
      if (__any(b0 | b1 | b2 | b3)) {
          if (__any(b0)) attn_safe(rs, kvo, lane, q[0], q[1], o0, mr0);
          if (__any(b1)) attn_safe(rs, kvo, lane, q[2], q[3], o1, mr1);
          if (__any(b2)) attn_safe(rs, kvo, lane, q[4], q[5], o2, mr2);
          if (__any(b3)) attn_safe(rs, kvo, lane, q[6], q[7], o3, mr3);
      } }
    while (__hip_atomic_load((LAS unsigned*)(lds + L_CNT + 12), __ATOMIC_RELAXED, __HIP_MEMORY_SCOPE_WORKGROUP) < 8u) __builtin_amdgcn_s_sleep(1);
    asm volatile("" ::: "memory");
    { bf16x8 f[8] = {pack8(o0, 0), pack8(o1, 0), pack8(o2, 0), pack8(o3, 0), pack8(o0, 1), pack8(o1, 1), pack8(o2, 1), pack8(o3, 1)};
#pragma unroll
      for (int i = 0; i < 8; ++i) PIN(f[i]);
      lptr mb = lds + L_MB + w * 1024 + vo;
#pragma unroll
      for (int i = 0; i < 8; ++i) *(LAS bf16x8*)(mb + i * 8192) = f[i];
      if (hh) { lptr ml = lds + L_ML + w * 1024 + r * 8;
          *(LAS f32x2*)(ml) = (f32x2){mr0, o0[12]}; *(LAS f32x2*)(ml + 256) = (f32x2){mr1, o1[12]}; *(LAS f32x2*)(ml + 512) = (f32x2){mr2, o2[12]}; *(LAS f32x2*)(ml + 768) = (f32x2){mr3, o3[12]}; } }
    wait_vm<8>();
    bar();
    { const int qi = w & 3, ks = w >> 2;
      bf16x8 fr[8]; f32x2 ml[8];
#pragma unroll
      for (int s = 0; s < 8; ++s) fr[s] = lds16(lds + L_MB + w * 8192 + s * 1024 + vo);
#pragma unroll
      for (int s = 0; s < 8; ++s) ml[s] = *(LAS f32x2*)(lds + L_ML + s * 1024 + qi * 256 + r * 8);
      u32x4 selw;
      { const int x = r - 16 * ks - 4 * hh;
        const unsigned one = (x & 1) ? 0x3F800000u : 0x00003F80u;
        selw[0] = (x >> 1) == 0 ? one : 0u; selw[1] = (x >> 1) == 1 ? one : 0u; selw[2] = (x >> 1) == 4 ? one : 0u; selw[3] = (x >> 1) == 5 ? one : 0u; }
      const bf16x8 sel = __builtin_bit_cast(bf16x8, selw);
      f32x16 d0 = MFMA(sel, fr[0], zero), d1 = MFMA(sel, fr[1], zero);
      d0 = MFMA(sel, fr[2], d0); d1 = MFMA(sel, fr[3], d1); d0 = MFMA(sel, fr[4], d0); d1 = MFMA(sel, fr[5], d1); d0 = MFMA(sel, fr[6], d0); d1 = MFMA(sel, fr[7], d1);
      bool same = true;
#pragma unroll
      for (int s = 1; s < 8; ++s) same = same & (ml[s][0] == ml[0][0]);
      float z[8], l = ((ml[0][1] + ml[1][1]) + (ml[2][1] + ml[3][1])) + ((ml[4][1] + ml[5][1]) + (ml[6][1] + ml[7][1]));
#pragma unroll
      for (int jj = 0; jj < 8; ++jj) z[jj] = ks ? d0[8 + jj] + d1[8 + jj] : d0[jj] + d1[jj];
      if (!__all(same)) {
          float M = ml[0][0];
#pragma unroll
          for (int s = 1; s < 8; ++s) M = __builtin_fmaxf(M, ml[s][0]);
          l = 0.f;
#pragma unroll
          for (int jj = 0; jj < 8; ++jj) z[jj] = 0.f;
#pragma unroll
          for (int s = 0; s < 8; ++s) { const float f = __builtin_amdgcn_exp2f(ml[s][0] - M); l += f * ml[s][1]; const u32x4 u = __builtin_bit_cast(u32x4, fr[s]);
#pragma unroll
              for (int jj = 0; jj < 4; ++jj) { z[2 * jj] += f * __uint_as_float(u[jj] << 16); z[2 * jj + 1] += f * __uint_as_float(u[jj] & 0xffff0000u); } }
      }
      const float inv = __builtin_amdgcn_rcpf(l);
      u32x4 u = {pk(z[0] * inv, z[1] * inv), pk(z[2] * inv, z[3] * inv), pk(z[4] * inv, z[5] * inv), pk(z[6] * inv, z[7] * inv)};
      if (ks && hh) { u[2] = 0; u[3] = 0; }
      st_dev(rs, WS_O + (((qb * 4 + qi) * 16 + h) * 2 + ks) * 1024 + vo, __builtin_bit_cast(bf16x8, u)); }
    wait_vm<0>();
    asm volatile("s_barrier" ::: "memory");
    if (tid == 0) flag_set(p.ws, F_OA + qb * 128 + h * 8, tag);
    if (has_tile) {
#pragma unroll
        for (int i = 0; i < 8; ++i) *(LAS u32x4*)(lds + L_OB + w * 8192 + i * 1024 + vo) = ob[i];
        bar();
    }
    if (has_tile) {
        const int blk = (lane >> 4) & 1, q4 = (lane & 15) >> 2, pp = lane & 3;
        const int cb = (w >> 1) & 1, kh = w >> 2;
        const int wfo = (4 * hh + q4) * 128 + (((4 * cb + 2 * blk + (pp >> 1)) ^ ((q4 >> 1) << 2)) << 4) + 8 * (pp & 1);
        const int wlast = (kh && hh) ? 4 * 128 : 8 * 128;
        lptr ex = lds + L_EX + (w & 3) * 4096 + vo;
#define OUTPROJ(WB, OB) do { \
      _Pragma("unroll") for (int hi = 0; hi < 8; ++hi) { \
          acc = MFMA(ldtr8((WB) + (hi * 28) * 128, 8 * 128), lds16((OB) + (hi * 2) * 1024), acc); \
          acc2 = MFMA(ldtr8((WB) + (hi * 28 + 16) * 128, hi == 7 ? wlast : 8 * 128), lds16((OB) + (hi * 2 + 1) * 1024), acc2); } \
      _Pragma("unroll") for (int i = 0; i < 16; ++i) acc[i] += acc2[i]; } while (0)
#define OUTEX() do { if (kh) { _Pragma("unroll") for (int i = 0; i < 4; ++i) *(LAS f32x4*)(ex + i * 1024) = (f32x4){acc[4 * i], acc[4 * i + 1], acc[4 * i + 2], acc[4 * i + 3]}; } } while (0)
#define OUTSTORE(COL) do { \
      if (!kh) { lptr tw = lds + L_TR + ((w & 1) * 32 + r) * 144 + cb * 64 + hh * 8; \
          _Pragma("unroll") for (int i = 0; i < 4; ++i) { const f32x4 y = *(LAS f32x4*)(ex + i * 1024); \
              *(LAS u32x2*)(tw + i * 16) = (u32x2){pk(acc[4 * i] + y[0], acc[4 * i + 1] + y[1]), pk(acc[4 * i + 2] + y[2], acc[4 * i + 3] + y[3])}; } } \
      bar(); \
      { const int row = w * 8 + (lane >> 3); const u32x4 v = *(LAS u32x4*)(lds + L_TR + row * 144 + (lane & 7) * 16); \
        __builtin_amdgcn_raw_buffer_store_b128(v, ors, (og * 64 + row) * OROW + (COL) + ocg * 128 + (lane & 7) * 16, 0, OUT_AUX); } } while (0)
        const rsrc_t ors = __builtin_amdgcn_make_buffer_rsrc(p.out, 0, 2048 * OROW, 0x00020000);
        u32x4 va[8];
        { f32x16 acc = {}, acc2 = {};
          OUTPROJ(lds + L_WB + kh * 8 * 28 * 128 + wfo, lds + L_OB + (w & 1) * 32768 + kh * 16384 + vo);
          bar();
          if (w < 7) { const char* wt = p.woa + ocg * 128 + (size_t)(224 + w * 32) * XROW; const unsigned lt = lds0 + L_WAH + w * 4096;
#pragma unroll
              for (int a = 0; a < 4; ++a) glds(wt + a * 8 * XROW, wvoff, lt + a * 1024); }
          OUTEX();
          if (w == 0) flags_wait(p.ws, F_OA + (og >> 1) * 128 + (lane & 15) * 8, tag);
          bar();
          const unsigned oao = WS_O + og * 65536 + w * 8192 + vo;
#pragma unroll
          for (int i = 0; i < 8; ++i) va[i] = __builtin_amdgcn_raw_buffer_load_b128(rs, oao + i * 1024, 0, 16);
          OUTSTORE(896); }
        { lptr od = lds + (w < 4 ? L_OA0 + w * 8192 : L_OA1 + (w - 4) * 8192) + vo;
#pragma unroll
          for (int i = 0; i < 8; ++i) *(LAS u32x4*)(od + i * 1024) = va[i];
          wait_vm<0>(); bar();
          f32x16 acc = {}, acc2 = {};
          OUTPROJ(lds + L_WA + kh * 8 * 28 * 128 + wfo, lds + ((w & 1) ? L_OA1 : L_OA0) + kh * 16384 + vo);
          OUTEX();
          bar();
          OUTSTORE(0); }
    }
}

extern "C" void kernel_launch(void* const* d_in, const int* in_sizes, int n_in, void* d_out, int out_size, void* d_ws, size_t ws_size, hipStream_t stream) {
    Params p;
    p.xa = (const char*)d_in[0]; p.wa = (const char*)d_in[1]; p.woa = (const char*)d_in[2];
    p.xb = (const char*)d_in[3]; p.wb = (const char*)d_in[4]; p.wob = (const char*)d_in[5];
    p.out = (char*)d_out; p.ws = (char*)d_ws;
    k_fused<<<256, 512, 0, stream>>>(p);
}
```
